```python
import torch

def _softmax(x, axis=-1):
    return torch.softmax(x, dim=axis)
def _gelu(x):
    return torch.nn.functional.gelu(x, approximate="tanh")
def _silu(x):
    return torch.nn.functional.silu(x)
def _sigmoid(x):
    return torch.sigmoid(x)
def _layernorm(x, axis=-1, eps=1e-5):
    m = x.mean(dim=axis, keepdim=True)
    v = x.var(dim=axis, keepdim=True, unbiased=False)
    return (x - m) / torch.sqrt(v + eps)
def _scatter_add(tgt, idx, src, axis=0):
    return tgt.clone().index_add_(axis, idx, src)
def _segment_sum(src, seg, n):
    out = torch.zeros((n,) + src.shape[1:], dtype=src.dtype)
    return out.index_add_(0, seg, src)
def _pad(x, pw):
    flat = tuple(v for pair in reversed(pw) for v in pair)
    return torch.nn.functional.pad(x, flat)

kernel_name = 'concat_heads__mha_block_cached__gqa_block_cached__decode'

def setup_inputs():

    _w = (lambda a: (lambda t: t.to(torch.bfloat16) if t.is_floating_point() else t)(torch.as_tensor(a)))
    def _A_setup():
        B, Sq, D = 8, 1, 512
        H, Sk, Dh = 32, 4096, 128
        return {
            "x": _w((lambda *s: torch.randn(*s, dtype=torch.bfloat16) * 0.8)(B, Sq, D)),
            "k_cache": _w((lambda *s: torch.randn(*s, dtype=torch.bfloat16) * 0.8)(B, H, Sk, Dh)),
            "v_cache": _w((lambda *s: torch.randn(*s, dtype=torch.bfloat16) * 0.05)(B, H, Sk, Dh)),
            "wq": _w((lambda *s: torch.randn(*s, dtype=torch.bfloat16) * 0.8)(D, H * Dh) * (D ** -0.5)),
            "wo": _w((lambda *s: torch.randn(*s, dtype=torch.bfloat16) * 0.05)(H * Dh, D) * ((H * Dh) ** -0.5)),
        }
    _A_in = _A_setup()

    def _B_setup():
        B, Sq, D = 8, 1, 512
        Hq, Hkv, Sk, Dh = 32, 8, 4096, 128
        assert Hq % Hkv == 0, (Hq, Hkv)
        return {
            "x": _w((lambda *s: torch.randn(*s, dtype=torch.bfloat16) * 0.8)(B, Sq, D)),
            "k_cache": _w((lambda *s: torch.randn(*s, dtype=torch.bfloat16) * 0.8)(B, Hkv, Sk, Dh)),
            "v_cache": _w((lambda *s: torch.randn(*s, dtype=torch.bfloat16) * 0.05)(B, Hkv, Sk, Dh)),
            "wq": _w((lambda *s: torch.randn(*s, dtype=torch.bfloat16) * 0.8)(D, Hq * Dh) * (D ** -0.5)),
            "wo": _w((lambda *s: torch.randn(*s, dtype=torch.bfloat16) * 0.05)(Hq * Dh, D) * ((Hq * Dh) ** -0.5)),
        }
    _B_in = _B_setup()

    return {
        "A_x": _A_in["x"],
        "A_k_cache": _A_in["k_cache"],
        "A_v_cache": _A_in["v_cache"],
        "A_wq": _A_in["wq"],
        "A_wo": _A_in["wo"],
        "B_x": _B_in["x"],
        "B_k_cache": _B_in["k_cache"],
        "B_v_cache": _B_in["v_cache"],
        "B_wq": _B_in["wq"],
        "B_wo": _B_in["wo"],
    }

def reference(A_x, A_k_cache, A_v_cache, A_wq, A_wo, B_x, B_k_cache, B_v_cache, B_wq, B_wo):
    def _A_ref(x, k_cache, v_cache, wq, wo):
        B, Sq, D = x.shape
        H, Dh = 32, 128
        q = (lambda x, *d: x.permute(*d))((x @ wq).reshape(B, Sq, H, Dh), 0, 2, 1, 3)
        scores = torch.einsum("bhqd,bhkd->bhqk", q, k_cache) * (Dh ** -0.5)
        p = _softmax(scores, axis=-1)
        o = torch.einsum("bhqk,bhkd->bhqd", p, v_cache)
        return (lambda x, *d: x.permute(*d))(o, 0, 2, 1, 3).reshape(B, Sq, H * Dh) @ wo

    def _B_ref(x, k_cache, v_cache, wq, wo):
        B, Sq, D = x.shape
        Hq, Dh = 32, 128
        Hkv = k_cache.shape[1]
        rep = Hq // Hkv
        q = (lambda x, *d: x.permute(*d))((x @ wq).reshape(B, Sq, Hq, Dh), 0, 2, 1, 3)
        k = (lambda x, n, axis: torch.repeat_interleave(x, n, dim=axis))(k_cache, rep, axis=1)
        v = (lambda x, n, axis: torch.repeat_interleave(x, n, dim=axis))(v_cache, rep, axis=1)
        scores = torch.einsum("bhqd,bhkd->bhqk", q, k) * (Dh ** -0.5)
        p = _softmax(scores, axis=-1)
        o = torch.einsum("bhqk,bhkd->bhqd", p, v)
        return (lambda x, *d: x.permute(*d))(o, 0, 2, 1, 3).reshape(B, Sq, Hq * Dh) @ wo

    a = _A_ref(A_x, A_k_cache, A_v_cache, A_wq, A_wo)
    b = _B_ref(B_x, B_k_cache, B_v_cache, B_wq, B_wo)
    return torch.concatenate([a, b], axis=-1)
```

```cpp
#include <hip/hip_runtime.h>
#include <hip/hip_bf16.h>
#include <stdint.h>

typedef unsigned u32x4 __attribute__((ext_vector_type(4)));
typedef unsigned u32x2 __attribute__((ext_vector_type(2)));
typedef float f32x4 __attribute__((ext_vector_type(4)));
typedef short s16x4 __attribute__((ext_vector_type(4)));
typedef __bf16 bf16x8 __attribute__((ext_vector_type(8)));
typedef __amdgpu_buffer_rsrc_t rsrc_t;
#define SB() __builtin_amdgcn_sched_barrier(0)
#define AUX_NT 2
#define AUX_SC1 16

constexpr int NB = 8, NH = 32, SK = 4096, DH = 128, DM = 512;
constexpr float CSC = 0.088388347648318440f * 1.4426950408889634f;
constexpr float NEG_BIG = -3.4028234663852886e38f;

__device__ __forceinline__ rsrc_t mk(const void* p, unsigned bytes) { return __builtin_amdgcn_make_buffer_rsrc((void*)p, 0, bytes, 0x00020000); }
template <int AUX> __device__ __forceinline__ u32x4 ld128(rsrc_t r, unsigned voff, unsigned soff) { return __builtin_amdgcn_raw_buffer_load_b128(r, voff, soff, AUX); }
__device__ __forceinline__ f32x4 mfma32(u32x4 a, u32x4 b, f32x4 c) {
  return __builtin_amdgcn_mfma_f32_16x16x32_bf16(__builtin_bit_cast(bf16x8, a), __builtin_bit_cast(bf16x8, b), c, 0, 0, 0);
}
__device__ __forceinline__ f32x4 mfma16(unsigned a0, unsigned a1, unsigned b0, unsigned b1, f32x4 c) {
  u32x2 a = {a0, a1}, b = {b0, b1};
  return __builtin_amdgcn_mfma_f32_16x16x16bf16_1k(__builtin_bit_cast(s16x4, a), __builtin_bit_cast(s16x4, b), c, 0, 0, 0);
}
typedef __bf16 bf16x2 __attribute__((ext_vector_type(2)));
typedef float f32x2 __attribute__((ext_vector_type(2)));
__device__ __forceinline__ unsigned cvtpk(float lo, float hi) {
  f32x2 v = {lo, hi};
  return __builtin_bit_cast(unsigned, __builtin_convertvector(v, bf16x2));
}
template <int CTRL> __device__ __forceinline__ float dpp(float x) {
  return __builtin_bit_cast(float, __builtin_amdgcn_mov_dpp(__builtin_bit_cast(int, x), CTRL, 0xf, 0xf, true));
}
__device__ __forceinline__ float xrow_max(float x) {
  auto s = __builtin_amdgcn_permlane16_swap(__float_as_uint(x), __float_as_uint(x), false, false);
  x = fmaxf(__uint_as_float(s[0]), __uint_as_float(s[1]));
  auto t = __builtin_amdgcn_permlane32_swap(__float_as_uint(x), __float_as_uint(x), false, false);
  return fmaxf(__uint_as_float(t[0]), __uint_as_float(t[1]));
}
__device__ __forceinline__ float xrow_sum(float x) {
  auto s = __builtin_amdgcn_permlane16_swap(__float_as_uint(x), __float_as_uint(x), false, false);
  x = __uint_as_float(s[0]) + __uint_as_float(s[1]);
  auto t = __builtin_amdgcn_permlane32_swap(__float_as_uint(x), __float_as_uint(x), false, false);
  return __uint_as_float(t[0]) + __uint_as_float(t[1]);
}

constexpr size_t WS_Q = 0;
constexpr size_t WS_PA = WS_Q + 2 * 256 * 256;
constexpr size_t WS_MLA = WS_PA + 256 * 4 * 256;
constexpr size_t WS_PB = WS_MLA + 256 * 4 * 8;
constexpr size_t WS_MLB = WS_PB + 256 * 16 * 256;
constexpr size_t WS_OUTP = WS_MLB + 256 * 16 * 8;

__global__ void __launch_bounds__(256) k_qproj(const uint16_t* xA, const uint16_t* wqA, const uint16_t* xB, const uint16_t* wqB, unsigned char* ws) {
  __shared__ float qpart[4][128];
  const int lane = threadIdx.x & 63, wave = __builtin_amdgcn_readfirstlane(threadIdx.x >> 6);
  const int br = blockIdx.x >> 8, j = blockIdx.x & 255, b = j >> 5, h = j & 31;
  const uint16_t* x = br ? xB : xA; const uint16_t* wq = br ? wqB : wqA;
  const int g = lane >> 4, c = lane & 15;
  rsrc_t rx = mk(x + b * DM, DM * 2), rw = mk(wq + h * DH, (unsigned)(DM * NH * DH * 2 - h * DH * 2));
  u32x4 xr[4];
#pragma unroll
  for (int t = 0; t < 4; ++t) xr[t] = ld128<0>(rx, (128 * wave + 32 * g) * 2 + t * 16, 0);
  const int jj = c & 3;
  const unsigned selL0 = jj == 0 ? 0x0c0c0100u : jj == 1 ? 0x01000c0cu : 0x0c0c0c0cu, selL1 = jj == 2 ? 0x0c0c0100u : jj == 3 ? 0x01000c0cu : 0x0c0c0c0cu;
  const unsigned selH0 = jj == 0 ? 0x0c0c0302u : jj == 1 ? 0x03020c0cu : 0x0c0c0c0cu, selH1 = jj == 2 ? 0x0c0c0302u : jj == 3 ? 0x03020c0cu : 0x0c0c0c0cu;
  f32x4 lo = {0, 0, 0, 0}, hi = {0, 0, 0, 0};
  const unsigned vo = (unsigned)(128 * wave + 32 * g) * 8192u + c * 16;
#pragma unroll
  for (int i = 0; i < 32; ++i) {
    u32x4 w = ld128<0>(rw, vo, i * 8192);
    unsigned xd = xr[i >> 3][(i >> 1) & 3];
    unsigned a0 = __builtin_amdgcn_perm(0u, xd, (i & 1) ? selH0 : selL0), a1 = __builtin_amdgcn_perm(0u, xd, (i & 1) ? selH1 : selL1);
    lo = mfma16(a0, a1, w[0], w[1], lo); hi = mfma16(a0, a1, w[2], w[3], hi);
  }
  if (g == 0) { *(f32x4*)&qpart[wave][8 * c] = lo; *(f32x4*)&qpart[wave][8 * c + 4] = hi; }
  __syncthreads();
  if (threadIdx.x < 64) {
    const int k = 2 * threadIdx.x;
    float s0 = (qpart[0][k] + qpart[1][k]) + (qpart[2][k] + qpart[3][k]);
    float s1 = (qpart[0][k + 1] + qpart[1][k + 1]) + (qpart[2][k + 1] + qpart[3][k + 1]);
    ((unsigned*)(ws + WS_Q))[(br * 256 + j) * 64 + threadIdx.x] = cvtpk(s0, s1);
  }
}

constexpr int U = 8;
struct State { float m, mc, l; f32x4 lo, hi; };

__device__ __forceinline__ void roll(u32x4 (&K)[U], u32x4 (&V)[U], const u32x4 Qf, State& st, rsrc_t rk, rsrc_t rv, unsigned so,
                                     unsigned kv, unsigned vv, unsigned mask0, unsigned mask1, int lane) {
  float s[U];
  f32x4 d[U];
  const f32x4 z = {0, 0, 0, 0};
#pragma unroll
  for (int i = 0; i < U; ++i) {
    d[i] = mfma32(K[i], Qf, z);
    K[i] = ld128<AUX_NT>(rk, kv + (i & 3) * 1024, so + (i >> 2) * 4096);
    if (i > 0) s[i - 1] = (dpp<0x00>(d[i - 1][0]) + dpp<0x55>(d[i - 1][1])) + (dpp<0xAA>(d[i - 1][2]) + dpp<0xFF>(d[i - 1][3]));
    SB();
  }
  s[U - 1] = (dpp<0x00>(d[U - 1][0]) + dpp<0x55>(d[U - 1][1])) + (dpp<0xAA>(d[U - 1][2]) + dpp<0xFF>(d[U - 1][3]));
  float mx = s[0];
#pragma unroll
  for (int i = 1; i < U; ++i) mx = fmaxf(mx, s[i]);
  if (__builtin_amdgcn_ballot_w64(mx > st.m) != 0) {
    const float mn = fmaxf(st.m, xrow_max(mx));
    const float alpha = __builtin_amdgcn_exp2f((st.m - mn) * CSC);
    st.l *= alpha; st.m = mn; st.mc = mn * CSC;
    const float a2 = __builtin_bit_cast(float, __builtin_amdgcn_ds_bpermute((lane >> 4) * 16, __builtin_bit_cast(int, alpha)));
    st.lo *= a2; st.hi *= a2;
  }
  SB();
#pragma unroll
  for (int i = 0; i < U; ++i) {
    const float p = __builtin_amdgcn_exp2f(fmaf(s[i], CSC, -st.mc));
    st.l += p;
    const unsigned t = cvtpk(p, p);
    const unsigned a0 = t & mask0, a1 = t & mask1;
    st.lo = mfma16(a0, a1, V[i][0], V[i][1], st.lo);
    st.hi = mfma16(a0, a1, V[i][2], V[i][3], st.hi);
    V[i] = ld128<AUX_NT>(rv, vv + (i & 3) * 1024, so + (i >> 2) * 4096);
    SB();
  }
}

__global__ void __launch_bounds__(256, 1) k_attn(const uint16_t* KA, const uint16_t* VA, const uint16_t* KB, const uint16_t* VB, unsigned char* ws) {
  const int lane = threadIdx.x & 63, wave = __builtin_amdgcn_readfirstlane(threadIdx.x >> 6);
  const int j = blockIdx.x, b = j >> 5, h = j & 31;
  const int g = lane >> 4, c = lane & 15;
  const unsigned kv = ((lane >> 2) & 3) * 256 + (4 * g + (lane & 3)) * 16, vv = lane * 16;
  const int jj = c & 3;
  const unsigned mask0 = jj == 0 ? 0x0000ffffu : jj == 1 ? 0xffff0000u : 0u, mask1 = jj == 2 ? 0x0000ffffu : jj == 3 ? 0xffff0000u : 0u;
  constexpr unsigned AB = 1u << 20, BB = 1u << 18, TILE = U * 1024;
  rsrc_t rkA = mk((const char*)KA + (size_t)j * AB, AB), rvA = mk((const char*)VA + (size_t)j * AB, AB);
  rsrc_t rkB = mk((const char*)KB + (size_t)j * BB, BB), rvB = mk((const char*)VB + (size_t)j * BB, BB);
  const u32x4 QA = *(const u32x4*)(ws + WS_Q + j * 256 + (4 * g + jj) * 16);
  const u32x4 QB = *(const u32x4*)(ws + WS_Q + (256 + (j & ~3) + (c >> 2)) * 256 + (4 * g + jj) * 16);
  u32x4 K[U], V[U];
  unsigned so = wave * TILE;
  SB();
#pragma unroll
  for (int i = 0; i < U; ++i) { K[i] = ld128<AUX_NT>(rkA, kv + (i & 3) * 1024, so + (i >> 2) * 4096); SB(); }
#pragma unroll
  for (int i = 0; i < U; ++i) { V[i] = ld128<AUX_NT>(rvA, vv + (i & 3) * 1024, so + (i >> 2) * 4096); SB(); }
  State st; st.m = NEG_BIG; st.mc = NEG_BIG * CSC; st.l = 0.f; st.lo = f32x4{0, 0, 0, 0}; st.hi = st.lo;
  constexpr int NTA = AB / (TILE * 4), NTB = BB / (TILE * 4);
  u32x4 Qf = QA;
#pragma unroll 1
  for (int t = 0; t < NTA + NTB; ++t) {
    const int n = t + 1;
    const bool nA = n < NTA;
    rsrc_t rk = nA ? rkA : rkB, rv = nA ? rvA : rvB;
    so = wave * TILE + (nA ? n : n - NTA) * (TILE * 4);
    roll(K, V, Qf, st, rk, rv, so, kv, vv, mask0, mask1, lane);
    if (n == NTA || n == NTA + NTB) {
      const float lt = xrow_sum(st.l);
      u32x4 pk = {cvtpk(st.lo[0], st.lo[1]), cvtpk(st.lo[2], st.lo[3]), cvtpk(st.hi[0], st.hi[1]), cvtpk(st.hi[2], st.hi[3])};
      if (n == NTA) {
        if (g == 0) *(u32x4*)(ws + WS_PA + (j * 4 + wave) * 256 + c * 16) = pk;
        if (lane == 0) *(float2*)(ws + WS_MLA + (j * 4 + wave) * 8) = make_float2(st.mc, lt);
      } else {
        const int row = 4 * (h & 3) + wave;
        *(u32x4*)(ws + WS_PB + (((j & ~3) + g) * 16 + row) * 256 + c * 16) = pk;
        if (g == 0 && jj == 0) *(float2*)(ws + WS_MLB + (((j & ~3) + (c >> 2)) * 16 + row) * 8) = make_float2(st.mc, lt);
      }
      st.m = NEG_BIG; st.mc = NEG_BIG * CSC; st.l = 0.f; st.lo = f32x4{0, 0, 0, 0}; st.hi = st.lo;
      Qf = QB;
    }
  }
}

__global__ void __launch_bounds__(256) k_proj(const uint16_t* woA, const uint16_t* woB, unsigned char* ws) {
  const int lane = threadIdx.x & 63, wave = __builtin_amdgcn_readfirstlane(threadIdx.x >> 6);
  const int br = blockIdx.x >> 8, j = blockIdx.x & 255, h = j & 31;
  const int g = lane >> 4, c = lane & 15;
  const uint16_t* wo = br ? woB : woA;
  rsrc_t rw = mk(wo + (size_t)h * DH * DM, DH * DM * 2);
  const int nrow = br ? 16 : 4;
  const unsigned char* pp = ws + (br ? WS_PB : WS_PA) + (size_t)(j * nrow + (c & (nrow - 1))) * 256;
  const float2 ml = *(const float2*)(ws + (br ? WS_MLB : WS_MLA) + (size_t)(j * nrow + (c & (nrow - 1))) * 8);
  const bool valid = c < nrow;
  float M = ml.x;
  M = fmaxf(M, dpp<0xB1>(M)); M = fmaxf(M, dpp<0x4E>(M)); M = fmaxf(M, dpp<0x141>(M)); M = fmaxf(M, dpp<0x128>(M));
  float wt = valid ? __builtin_amdgcn_exp2f(ml.x - M) : 0.f;
  float L = wt * ml.y;
  L += dpp<0xB1>(L); L += dpp<0x4E>(L); L += dpp<0x141>(L); L += dpp<0x128>(L);
  wt = wt / L;
  float w4[4];
#pragma unroll
  for (int t = 0; t < 4; ++t) w4[t] = __builtin_bit_cast(float, __builtin_amdgcn_ds_bpermute((4 * g + t) * 4, __builtin_bit_cast(int, wt)));
  f32x4 D[8];
#pragma unroll
  for (int d = 0; d < 8; ++d) D[d] = f32x4{0, 0, 0, 0};
#pragma unroll
  for (int s = 0; s < 4; ++s) {
    const u32x4 A = *(const u32x4*)(pp + (4 * s + g) * 16);
    u32x4 W[8];
#pragma unroll
    for (int i = 0; i < 8; ++i) W[i] = ld128<0>(rw, (unsigned)(8 * g) * 1024u + wave * 256 + c * 16, (32 * s + i) * 1024);
#pragma unroll
    for (int d = 0; d < 8; ++d) {
      u32x4 T;
#pragma unroll
      for (int t = 0; t < 4; ++t) T[t] = __builtin_amdgcn_perm(W[2 * t + 1][d >> 1], W[2 * t][d >> 1], (d & 1) ? 0x07060302u : 0x05040100u);
      D[d] = mfma32(A, T, D[d]);
    }
  }
  float o[8];
#pragma unroll
  for (int d = 0; d < 8; ++d) o[d] = xrow_sum((D[d][0] * w4[0] + D[d][1] * w4[1]) + (D[d][2] * w4[2] + D[d][3] * w4[3]));
  if (g == 0) {
    float* op = (float*)(ws + WS_OUTP) + (size_t)(br * 256 + j) * 512 + wave * 128 + c * 8;
    *(f32x4*)op = f32x4{o[0], o[1], o[2], o[3]}; *(f32x4*)(op + 4) = f32x4{o[4], o[5], o[6], o[7]};
  }
}

__global__ void __launch_bounds__(256) k_final(const unsigned char* ws, uint16_t* out) {
  const int idx = blockIdx.x * 256 + threadIdx.x;
  const int b = idx >> 9, col = (idx & 511) * 2, br = col >> 9, n = col & 511;
  const float* op = (const float*)(ws + WS_OUTP) + (size_t)(br * 256 + b * 32) * 512 + n;
  float s0 = 0.f, s1 = 0.f;
#pragma unroll
  for (int h = 0; h < 32; ++h) { const float2 v = *(const float2*)(op + h * 512); s0 += v.x; s1 += v.y; }
  ((unsigned*)out)[idx] = cvtpk(s0, s1);
}

extern "C" void kernel_launch(void* const* d_in, const int* in_sizes, int n_in, void* d_out, int out_size, void* d_ws, size_t ws_size, hipStream_t stream) {
  const uint16_t* xA = (const uint16_t*)d_in[0]; const uint16_t* KA = (const uint16_t*)d_in[1]; const uint16_t* VA = (const uint16_t*)d_in[2];
  const uint16_t* wqA = (const uint16_t*)d_in[3]; const uint16_t* woA = (const uint16_t*)d_in[4];
  const uint16_t* xB = (const uint16_t*)d_in[5]; const uint16_t* KB = (const uint16_t*)d_in[6]; const uint16_t* VB = (const uint16_t*)d_in[7];
  const uint16_t* wqB = (const uint16_t*)d_in[8]; const uint16_t* woB = (const uint16_t*)d_in[9];
  unsigned char* ws = (unsigned char*)d_ws;
  k_qproj<<<512, 256, 0, stream>>>(xA, wqA, xB, wqB, ws);
  k_attn<<<256, 256, 0, stream>>>(KA, VA, KB, VB, ws);
  k_proj<<<512, 256, 0, stream>>>(woA, woB, ws);
  k_final<<<16, 256, 0, stream>>>(ws, (uint16_t*)d_out);
}
```

```cpp
#include <hip/hip_runtime.h>
#include <hip/hip_bf16.h>
#include <stdint.h>

typedef unsigned u32x4 __attribute__((ext_vector_type(4)));
typedef unsigned u32x2 __attribute__((ext_vector_type(2)));
typedef float f32x4 __attribute__((ext_vector_type(4)));
typedef float f32x2 __attribute__((ext_vector_type(2)));
typedef short s16x4 __attribute__((ext_vector_type(4)));
typedef __bf16 bf16x8 __attribute__((ext_vector_type(8)));
typedef __bf16 bf16x2 __attribute__((ext_vector_type(2)));
typedef __amdgpu_buffer_rsrc_t rsrc_t;
#define LAS __attribute__((address_space(3)))
#define SB() __builtin_amdgcn_sched_barrier(0)
#define AUX_NT 2
#define AUX_SC1 16
#define DEPTH 16
#define HPRIO 3
#define D0 16
#define QD 32
#define RATE 7.1
#define GG 4
#define QP 16
#define P2END 22
#define T1 20

constexpr int DH = 128, DM = 512;
constexpr float CSC = 0.088388347648318440f * 1.4426950408889634f;
constexpr float NEG_BIG = -3.4028234663852886e38f;
constexpr unsigned SENT = 0xFFFFFFFFu;

__device__ __forceinline__ rsrc_t mk(const void* p, unsigned bytes) { return __builtin_amdgcn_make_buffer_rsrc((void*)p, 0, bytes, 0x00020000); }
template <int AUX> __device__ __forceinline__ u32x4 ld128(rsrc_t r, unsigned voff, unsigned soff) { return __builtin_amdgcn_raw_buffer_load_b128(r, voff, soff, AUX); }
__device__ __forceinline__ void st128(u32x4 v, rsrc_t r, unsigned off) { __builtin_amdgcn_raw_buffer_store_b128(v, r, off, 0, AUX_SC1); }
__device__ __forceinline__ f32x4 mfma32(u32x4 a, u32x4 b, f32x4 c) {
  return __builtin_amdgcn_mfma_f32_16x16x32_bf16(__builtin_bit_cast(bf16x8, a), __builtin_bit_cast(bf16x8, b), c, 0, 0, 0);
}
__device__ __forceinline__ f32x4 mfma16(unsigned a0, unsigned a1, unsigned b0, unsigned b1, f32x4 c) {
  u32x2 a = {a0, a1}, b = {b0, b1};
  return __builtin_amdgcn_mfma_f32_16x16x16bf16_1k(__builtin_bit_cast(s16x4, a), __builtin_bit_cast(s16x4, b), c, 0, 0, 0);
}
__device__ __forceinline__ unsigned cvtpk(float lo, float hi) {
  f32x2 v = {lo, hi};
  return __builtin_bit_cast(unsigned, __builtin_convertvector(v, bf16x2));
}
template <int CTRL> __device__ __forceinline__ float dpp(float x) {
  return __builtin_bit_cast(float, __builtin_amdgcn_mov_dpp(__builtin_bit_cast(int, x), CTRL, 0xf, 0xf, true));
}
__device__ __forceinline__ float xrow_max(float x) {
  auto s = __builtin_amdgcn_permlane16_swap(__float_as_uint(x), __float_as_uint(x), false, false);
  x = fmaxf(__uint_as_float(s[0]), __uint_as_float(s[1]));
  auto t = __builtin_amdgcn_permlane32_swap(__float_as_uint(x), __float_as_uint(x), false, false);
  return fmaxf(__uint_as_float(t[0]), __uint_as_float(t[1]));
}
__device__ __forceinline__ float xrow_sum(float x) {
  auto s = __builtin_amdgcn_permlane16_swap(__float_as_uint(x), __float_as_uint(x), false, false);
  x = __uint_as_float(s[0]) + __uint_as_float(s[1]);
  auto t = __builtin_amdgcn_permlane32_swap(__float_as_uint(x), __float_as_uint(x), false, false);
  return __uint_as_float(t[0]) + __uint_as_float(t[1]);
}
__device__ __forceinline__ unsigned nosent(unsigned v) { return v < 0xFFFFFFFEu ? v : 0xFFFFFFFEu; }
#define WAIT_LGKM() asm volatile("s_waitcnt lgkmcnt(0)" ::: "memory")
#define WAIT_VM0() asm volatile("s_waitcnt vmcnt(0)" ::: "memory")
template <int N> __device__ __forceinline__ void waitvm() { asm volatile("s_waitcnt vmcnt(%0)" :: "n"(N)); }
__device__ __forceinline__ unsigned now32() { return (unsigned)__builtin_amdgcn_s_memrealtime(); }
__device__ __forceinline__ void pace_until(unsigned target) {
#pragma unroll 1
  for (int spins = 0; spins < 4096 && (int)(now32() - target) < 0; ++spins) __builtin_amdgcn_s_sleep(1);
}

constexpr unsigned TAU = (unsigned)(1.048576 / RATE * 100 * 256 + 0.5);
constexpr unsigned TAU1 = TAU + TAU * 4 / 32;
constexpr unsigned TAU2 = TAU + TAU * 12 / ((P2END - 2) * 16);
constexpr unsigned PACE = (P2END - 2) * 16 * TAU2 / 256 / 104;
struct Pacer {
  unsigned t0, acc, step;
  __device__ __forceinline__ void gate() {
    const unsigned target = t0 + (acc >> 8);
#pragma unroll 1
    for (int spins = 0; spins < 4096 && (int)(now32() - target) < 0; ++spins) __builtin_amdgcn_s_sleep(1);
    acc += step;
  }
};

constexpr unsigned WS_EPOCH = 0;
constexpr unsigned WS_FLAG = 2048;
constexpr unsigned WS_QB = 4096;
constexpr unsigned WS_PB = WS_QB + 256 * 256;
constexpr unsigned WS_MLB = WS_PB + 256 * 1024;
constexpr unsigned WS_OUTP = WS_MLB + 256 * 64;
constexpr unsigned WS_END = WS_OUTP + 256 * 2048;
#define STAMP(k) do {} while (0)
#define STAMP_DECL do {} while (0)
#define STAMP_FLUSH() do {} while (0)
constexpr int L_QPA = 0;
constexpr int L_QPB = L_QPA + 2048;
constexpr int L_QFB = L_QPB + 2048;
constexpr int L_PB = L_QFB + 1024;
constexpr int L_MLB = L_PB + 8192;
constexpr int L_PA = L_MLB + 128;
constexpr int L_MLA = L_PA + 1024;
constexpr int L_SYNC = L_MLA + 32;
constexpr int L_WOA = L_SYNC + 32;
constexpr int L_END = L_WOA + 131072;
enum { S_QB_CNT = 0, S_QB_READY = 1, S_BP_CNT = 2, S_AP_CNT = 3, S_ML_SEEN = 4, S_QA_CNT = 5 };

__device__ __forceinline__ void lds_wait_ge(LAS unsigned* w, unsigned target) {
#pragma unroll 1
  for (unsigned spins = 0; spins < (1u << 22); ++spins) {
    const unsigned v = __builtin_amdgcn_readfirstlane(__hip_atomic_load(w, __ATOMIC_RELAXED, __HIP_MEMORY_SCOPE_WORKGROUP));
    if (v >= target) break;
    __builtin_amdgcn_s_sleep(1);
  }
  asm volatile("" ::: "memory");
}
__device__ __forceinline__ void lds_signal(LAS unsigned* w, int lane) {
  WAIT_LGKM();
  if (lane == 0) __hip_atomic_fetch_add(w, 1u, __ATOMIC_RELAXED, __HIP_MEMORY_SCOPE_WORKGROUP);
}

constexpr int U = 8;
constexpr unsigned TILE = U * 1024;
struct State { float m, mc, l; f32x4 lo, hi; };
__device__ __forceinline__ void st_init(State& st) { st.m = NEG_BIG; st.mc = NEG_BIG * CSC; st.l = 0.f; st.lo = f32x4{0, 0, 0, 0}; st.hi = st.lo; }
__device__ __forceinline__ float quad_diag(const f32x4 d) { return ((dpp<0x00>(d[0]) + dpp<0x55>(d[1])) + dpp<0xAA>(d[2])) + dpp<0xFF>(d[3]); }

template <bool RELOAD>
__device__ __forceinline__ void roll(u32x4 (&K)[U], u32x4 (&V)[U], const u32x4 Qf, State& st, rsrc_t rk, rsrc_t rv, unsigned so,
                                     unsigned kv, unsigned vv, unsigned mask0, unsigned mask1, int lane, const unsigned (&lo)[U], Pacer& pc) {
  float s[U];
  f32x4 d[U];
  const f32x4 z = {0, 0, 0, 0};
#pragma unroll
  for (int i = 0; i < U; ++i) {
    d[i] = mfma32(K[i], Qf, z);
    if (RELOAD) {
      if (i % GG == 0) pc.gate();
      if (DEPTH < 2 * U) waitvm<DEPTH - 1>();
      K[i] = ld128<AUX_NT>(rk, kv, so + lo[i]);
    }
    if (i > 0) s[i - 1] = quad_diag(d[i - 1]);
    if (RELOAD) SB();
  }
  s[U - 1] = quad_diag(d[U - 1]);
  float mx = s[0];
#pragma unroll
  for (int i = 1; i < U; ++i) mx = fmaxf(mx, s[i]);
  if (__builtin_amdgcn_ballot_w64(mx > st.m) != 0) {
    const float mn = fmaxf(st.m, xrow_max(mx));
    const float alpha = __builtin_amdgcn_exp2f((st.m - mn) * CSC);
    st.l *= alpha; st.m = mn; st.mc = mn * CSC;
    const float a2 = __builtin_bit_cast(float, __builtin_amdgcn_ds_bpermute((lane >> 4) * 16, __builtin_bit_cast(int, alpha)));
    st.lo *= a2; st.hi *= a2;
  }
  SB();
#pragma unroll
  for (int i = 0; i < U; ++i) {
    const float p = __builtin_amdgcn_exp2f(fmaf(s[i], CSC, -st.mc));
    st.l += p;
    const unsigned t = cvtpk(p, p);
    const unsigned a0 = t & mask0, a1 = t & mask1;
    st.lo = mfma16(a0, a1, V[i][0], V[i][1], st.lo);
    st.hi = mfma16(a0, a1, V[i][2], V[i][3], st.hi);
    if (RELOAD) {
      if (i % GG == 0) pc.gate();
      if (DEPTH < 2 * U) waitvm<DEPTH - 1>();
      V[i] = ld128<AUX_NT>(rv, vv, so + lo[i]);
      SB();
    }
  }
  SB();
}
template <int D, bool BAR = false>
__device__ __forceinline__ void issue_tile(u32x4 (&K)[U], u32x4 (&V)[U], rsrc_t rk, rsrc_t rv, unsigned so, unsigned kv, unsigned vv, const unsigned (&lo)[U], Pacer& pc) {
#pragma unroll
  for (int i = 0; i < U; ++i) { if (i % GG == 0) pc.gate(); waitvm<D - 1>(); K[i] = ld128<AUX_NT>(rk, kv, so + lo[i]); SB(); if (BAR && i == GG - 1) { __builtin_amdgcn_s_barrier(); SB(); } }
#pragma unroll
  for (int i = 0; i < U; ++i) { if (i % GG == 0) pc.gate(); waitvm<D - 1>(); V[i] = ld128<AUX_NT>(rv, vv, so + lo[i]); SB(); }
}

template <int D, int PC> struct QProj {
  static constexpr int NL = 32;
  u32x4 xr[NL / 8], w[NL];
  __device__ __forceinline__ void issue(const uint16_t* x, const uint16_t* wq, int b, int h, int r0, int g, int c, unsigned tslot) {
    rsrc_t rx = mk(x + b * DM, DM * 2), rw = mk(wq + h * DH, (unsigned)(DM * 4096 * 2 - h * DH * 2));
#pragma unroll
    for (int t = 0; t < NL / 8; ++t) { xr[t] = ld128<0>(rx, (NL * g) * 2 + t * 16, r0 * 2); SB(); }
    const unsigned vo = (unsigned)(NL * g) * 8192u + c * 16;
#pragma unroll
    for (int i = 0; i < NL; ++i) {
      if (PC && (i & 1) == 0) pace_until(tslot + i * PC);
      if (D < NL) waitvm<(D < NL ? D - 1 : 0)>();
      w[i] = ld128<AUX_NT>(rw, vo, (r0 + i) * 8192); SB();
    }
  }
  __device__ __forceinline__ void compute(f32x4& lo, f32x4& hi, int c) {
    const int jj = c & 3;
    const unsigned selL0 = jj == 0 ? 0x0c0c0100u : jj == 1 ? 0x01000c0cu : 0x0c0c0c0cu, selL1 = jj == 2 ? 0x0c0c0100u : jj == 3 ? 0x01000c0cu : 0x0c0c0c0cu;
    const unsigned selH0 = selL0 | (jj < 2 ? 0x02020202u & (jj == 0 ? 0x0000ffffu : 0xffff0000u) : 0u);
    const unsigned selH1 = selL1 | (jj >= 2 ? 0x02020202u & (jj == 2 ? 0x0000ffffu : 0xffff0000u) : 0u);
#pragma unroll
    for (int i = 0; i < NL; ++i) {
      const unsigned xd = xr[i >> 3][(i >> 1) & 3];
      const unsigned a0 = __builtin_amdgcn_perm(0u, xd, (i & 1) ? selH0 : selL0), a1 = __builtin_amdgcn_perm(0u, xd, (i & 1) ? selH1 : selL1);
      lo = mfma16(a0, a1, w[i][0], w[i][1], lo); hi = mfma16(a0, a1, w[i][2], w[i][3], hi);
    }
  }
};

__device__ __forceinline__ void wo_issue(u32x4 (&W)[8], rsrc_t rw, unsigned vo, int s, unsigned tslot) {
#pragma unroll
  for (int i = 0; i < 8; ++i) {
    if ((i & 1) == 0) pace_until(tslot + i * PACE);
    W[i] = ld128<AUX_NT>(rw, vo + (i & 3) * 1024, (32 * s + (i & 4)) * 1024); SB();
  }
}
__device__ __forceinline__ u32x4 wo_frag(const u32x4 (&W)[8], int d) {
  u32x4 T;
#pragma unroll
  for (int t = 0; t < 4; ++t) T[t] = __builtin_amdgcn_perm(W[2 * t + 1][d >> 1], W[2 * t][d >> 1], (d & 1) ? 0x07060302u : 0x05040100u);
  return T;
}
__device__ __forceinline__ u32x4 project4(const u32x4 (&A)[4], const u32x4 (&T)[32], const f32x4 m4, const f32x4 l4) {
  const float M = fmaxf(fmaxf(m4[0], m4[1]), fmaxf(m4[2], m4[3]));
  f32x4 w4;
#pragma unroll
  for (int t = 0; t < 4; ++t) w4[t] = __builtin_amdgcn_exp2f(m4[t] - M);
  const float L = (w4[0] * l4[0] + w4[1] * l4[1]) + (w4[2] * l4[2] + w4[3] * l4[3]);
  w4 *= 1.0f / L;
  float o[8];
#pragma unroll
  for (int d = 0; d < 8; ++d) {
    f32x4 D = {0, 0, 0, 0};
#pragma unroll
    for (int s = 0; s < 4; ++s) D = mfma32(A[s], T[s * 8 + d], D);
    o[d] = (D[0] * w4[0] + D[1] * w4[1]) + (D[2] * w4[2] + D[3] * w4[3]);
  }
  return u32x4{nosent(cvtpk(o[0], o[1])), nosent(cvtpk(o[2], o[3])), nosent(cvtpk(o[4], o[5])), nosent(cvtpk(o[6], o[7]))};
}

__global__ void __launch_bounds__(512, 2) k_fused(const uint16_t* xA, const uint16_t* KA, const uint16_t* VA, const uint16_t* wqA, const uint16_t* woA,
                                                  const uint16_t* xB, const uint16_t* KB, const uint16_t* VB, const uint16_t* wqB, const uint16_t* woB,
                                                  uint16_t* out, unsigned char* ws) {
  __shared__ __attribute__((aligned(16))) unsigned char smem[L_END];
  LAS unsigned char* lds = (LAS unsigned char*)smem;
  LAS unsigned* sync = (LAS unsigned*)(lds + L_SYNC);
  const int lane = threadIdx.x & 63, wave = __builtin_amdgcn_readfirstlane(threadIdx.x >> 6);
  const int j = blockIdx.x, b = j >> 5, h = j & 31;
  const int g = lane >> 4, c = lane & 15, jj = c & 3;
  STAMP_DECL;
  STAMP(0);
  if (wave < 4) {
    constexpr unsigned AB = 1u << 20, BB = 1u << 18;
    const unsigned kv = ((lane >> 2) & 3) * 256 + (4 * g + (lane & 3)) * 16, vv = lane * 16;
    rsrc_t rkA = mk((const char*)KA + (size_t)j * AB, AB), rvA = mk((const char*)VA + (size_t)j * AB, AB);
    u32x4 K[U], V[U], K0[U], V0[U], K1[U], V1[U];
    unsigned lo[U];
#pragma unroll
    for (int i = 0; i < U; ++i) lo[i] = (4 * ((i + (j >> 3)) & 7) + wave) * 1024;
    Pacer pc; pc.t0 = now32(); pc.acc = 0; pc.step = TAU1 * GG;
    unsigned so;
    issue_tile<D0, true>(K0, V0, rkA, rvA, 0, kv, vv, lo, pc);
    issue_tile<D0>(K1, V1, rkA, rvA, TILE * 4, kv, vv, lo, pc);
    pc.step = TAU2 * GG;
    issue_tile<D0>(K, V, rkA, rvA, TILE * 8, kv, vv, lo, pc);
    rsrc_t rkB = mk((const char*)KB + (size_t)j * BB, BB), rvB = mk((const char*)VB + (size_t)j * BB, BB);
    STAMP(5);
    lds_wait_ge(sync + S_QA_CNT, 4);
    u32x4 QA;
    {
      f32x4 ql[4], hi[4];
#pragma unroll
      for (int w = 0; w < 4; ++w) { ql[w] = *(LAS f32x4*)(lds + L_QPA + w * 512 + (4 * g + jj) * 32); hi[w] = *(LAS f32x4*)(lds + L_QPA + w * 512 + (4 * g + jj) * 32 + 16); }
      SB();
      const f32x4 l = (ql[0] + ql[1]) + (ql[2] + ql[3]), hh = (hi[0] + hi[1]) + (hi[2] + hi[3]);
      QA = u32x4{cvtpk(l[0], l[1]), cvtpk(l[2], l[3]), cvtpk(hh[0], hh[1]), cvtpk(hh[2], hh[3])};
    }
    STAMP(1);
    const unsigned mask0 = jj == 0 ? 0x0000ffffu : jj == 1 ? 0xffff0000u : 0u, mask1 = jj == 2 ? 0x0000ffffu : jj == 3 ? 0xffff0000u : 0u;
    State st, sv; st_init(st); st_init(sv);
    constexpr int NTA = AB / (TILE * 4), NTB = BB / (TILE * 4);
    u32x4 Qf = QA;
    roll<false>(K0, V0, Qf, st, rkA, rvA, 0, kv, vv, mask0, mask1, lane, lo, pc);
    STAMP(16);
    roll<true>(K, V, Qf, st, rkA, rvA, TILE * 12, kv, vv, mask0, mask1, lane, lo, pc);
    STAMP(17);
    roll<false>(K1, V1, Qf, st, rkA, rvA, 0, kv, vv, mask0, mask1, lane, lo, pc);
    STAMP(18);
#pragma unroll 1
    for (int p = 3; p < NTA + NTB; ++p) {
      const int n = p + 1;
      const bool nB = n >= T1 && n < T1 + NTB;
      rsrc_t rk = nB ? rkB : rkA, rv = nB ? rvB : rvA;
      pc.step = n < P2END ? TAU2 * GG : TAU * GG;
      if (n == NTA + NTB) { pc.step = 0; pc.acc = 0; }
      so = __builtin_amdgcn_readfirstlane((nB ? n - T1 : (n < T1 ? n : n - NTB)) * (TILE * 4));
      roll<true>(K, V, Qf, st, rk, rv, so, kv, vv, mask0, mask1, lane, lo, pc);
      STAMP(16 + p);
      if (n == T1) {
        sv = st; st_init(st);
        STAMP(3);
        lds_wait_ge(sync + S_QB_READY, 1);
        STAMP(4);
        Qf = *(LAS u32x4*)(lds + L_QFB + lane * 16);
      } else if (n == T1 + NTB) {
        const float lt = xrow_sum(st.l);
        *(LAS f32x4*)(lds + L_PB + wave * 2048 + lane * 32) = st.lo; *(LAS f32x4*)(lds + L_PB + wave * 2048 + lane * 32 + 16) = st.hi;
        if (g == 0 && jj == 0) { *(LAS float*)(lds + L_MLB + (c >> 2) * 32 + wave * 4) = st.mc; *(LAS float*)(lds + L_MLB + (c >> 2) * 32 + 16 + wave * 4) = lt; }
        lds_signal(sync + S_BP_CNT, lane);
        st = sv; Qf = QA;
      }
    }
    {
      const float lt = xrow_sum(st.l);
      if (g == 0) *(LAS u32x4*)(lds + L_PA + wave * 256 + c * 16) = u32x4{cvtpk(st.lo[0], st.lo[1]), cvtpk(st.lo[2], st.lo[3]), cvtpk(st.hi[0], st.hi[1]), cvtpk(st.hi[2], st.hi[3])};
      if (lane == 0) { *(LAS float*)(lds + L_MLA + wave * 4) = st.mc; *(LAS float*)(lds + L_MLA + 16 + wave * 4) = lt; }
      lds_signal(sync + S_AP_CNT, lane);
    }
    STAMP(2);
  } else {
    const int hw = wave - 4;
    __builtin_amdgcn_s_setprio(HPRIO);
    const unsigned th0 = now32();
    if (hw == 0 && lane < 8) sync[lane] = 0u;
    WAIT_LGKM();
    __builtin_amdgcn_s_barrier();
    SB();
    const unsigned tbase = th0 + 32 * TAU1 / 256 + 100;
    rsrc_t rws = mk(ws, WS_END);
    const unsigned op_off = WS_OUTP + j * 2048 + hw * 512 + (c >> 1) * 64 + (g & 1) * 32 + (c & 1) * 16;
    u32x2 ep;
    {
      QProj<QD, QP> qp;
      if (g < 2) st128(u32x4{SENT, SENT, SENT, SENT}, rws, op_off);
      ep = __builtin_amdgcn_raw_buffer_load_b64(rws, 0, WS_EPOCH + b * 256, AUX_SC1);
      SB();
      qp.issue(xA, wqA, b, h, 128 * hw, g, c, th0);
      f32x4 lo = {0, 0, 0, 0}, hi = {0, 0, 0, 0};
      qp.compute(lo, hi, c);
      if (g == 0) { *(LAS f32x4*)(lds + L_QPA + hw * 512 + c * 32) = lo; *(LAS f32x4*)(lds + L_QPA + hw * 512 + c * 32 + 16) = hi; }
    }
    lds_signal(sync + S_QA_CNT, lane);
    STAMP(10);
    {
      f32x4 lo = {0, 0, 0, 0}, hi = {0, 0, 0, 0};
      { QProj<32, PACE> qp; qp.issue(xB, wqB, b, h, 128 * hw, g, c, tbase); qp.compute(lo, hi, c); SB(); }
      if (g == 0) { *(LAS f32x4*)(lds + L_QPB + hw * 512 + c * 32) = lo; *(LAS f32x4*)(lds + L_QPB + hw * 512 + c * 32 + 16) = hi; }
    }
    WAIT_VM0();
    lds_signal(sync + S_QB_CNT, lane);
    const unsigned long long tag64 = (((unsigned long long)ep[1] << 32) | ep[0]) + 1ull;
    const u32x2 tag = {(unsigned)tag64, (unsigned)(tag64 >> 32)};
    if (hw == 0) {
      lds_wait_ge(sync + S_QB_CNT, 4);
      f32x2 s = {0, 0};
#pragma unroll
      for (int w = 0; w < 4; ++w) s += *(LAS f32x2*)(lds + L_QPB + w * 512 + lane * 8);
      __builtin_amdgcn_raw_buffer_store_b32(cvtpk(s[0], s[1]), rws, lane * 4, WS_QB + j * 256, AUX_SC1);
      WAIT_VM0();
      if (lane == 0) __builtin_amdgcn_raw_buffer_store_b64(tag, rws, 0, WS_FLAG + j * 8, AUX_SC1);
    }
    STAMP(1);
    const unsigned wvo = (unsigned)(8 * g) * 1024u + hw * 256 + c * 16;
    u32x4 T[32];
    {
      rsrc_t rwA = mk(woA + (size_t)h * DH * DM, DH * DM * 2), rwB = mk(woB + (size_t)h * DH * DM, DH * DM * 2);
      const unsigned ts = tbase + 38 * PACE;
      u32x4 W0[8], W1[8];
      wo_issue(W0, rwA, wvo, 0, ts);
#pragma unroll
      for (int s = 0; s < 8; s += 2) {
        if (s + 1 < 4) wo_issue(W1, rwA, wvo, s + 1, ts + (s + 1) * 8 * PACE); else wo_issue(W1, rwB, wvo, s + 1 - 4, ts + (s + 1) * 8 * PACE);
#pragma unroll
        for (int d = 0; d < 8; ++d) { if (s < 4) *(LAS u32x4*)(lds + L_WOA + hw * 32768 + (s * 8 + d) * 1024 + lane * 16) = wo_frag(W0, d); else T[(s - 4) * 8 + d] = wo_frag(W0, d); }
        SB();
        if (s + 2 < 4) wo_issue(W0, rwA, wvo, s + 2, ts + (s + 2) * 8 * PACE); else if (s + 2 < 8) wo_issue(W0, rwB, wvo, s + 2 - 4, ts + (s + 2) * 8 * PACE);
#pragma unroll
        for (int d = 0; d < 8; ++d) { if (s + 1 < 4) *(LAS u32x4*)(lds + L_WOA + hw * 32768 + ((s + 1) * 8 + d) * 1024 + lane * 16) = wo_frag(W1, d); else T[(s + 1 - 4) * 8 + d] = wo_frag(W1, d); }
        SB();
        if (s == 0 && hw == 1) {
          unsigned fo = (lane & 31) * 8;
#pragma unroll 1
          for (unsigned spins = 0; spins < (1u << 17); ++spins) {
            asm volatile("" : "+v"(fo) :: "memory");
            const u32x2 f = __builtin_amdgcn_raw_buffer_load_b64(rws, fo, WS_FLAG + b * 256, AUX_SC1);
            if (__builtin_amdgcn_ballot_w64(f[0] != tag[0] || f[1] != tag[1]) == 0) break;
            __builtin_amdgcn_s_sleep(64);
          }
          asm volatile("" ::: "memory");
          if (h == 0 && lane == 0) __builtin_amdgcn_raw_buffer_store_b64(tag, rws, 0, WS_EPOCH + b * 256, AUX_SC1);
          const u32x4 q = ld128<AUX_SC1>(rws, (c >> 2) * 256 + (4 * g + jj) * 16, WS_QB + (j & ~3) * 256);
          *(LAS u32x4*)(lds + L_QFB + lane * 16) = q;
          lds_signal(sync + S_QB_READY, lane);
          STAMP(3);
        }
      }
    }
    STAMP(2);
    lds_wait_ge(sync + S_BP_CNT, 4);
    STAMP(4);
    {
      const f32x4 m4 = *(LAS f32x4*)(lds + L_MLB + hw * 32), l4 = *(LAS f32x4*)(lds + L_MLB + hw * 32 + 16);
      const float M = fmaxf(fmaxf(m4[0], m4[1]), fmaxf(m4[2], m4[3]));
      f32x2 a = {0, 0}; float L = 0.f;
#pragma unroll
      for (int w = 0; w < 4; ++w) { const float e = __builtin_amdgcn_exp2f(m4[w] - M); L += e * l4[w]; a += e * *(LAS f32x2*)(lds + L_PB + w * 2048 + hw * 512 + lane * 8); }
      const unsigned slot = ((j & ~3) + hw) * 4 + (h & 3);
      __builtin_amdgcn_raw_buffer_store_b32(cvtpk(a[0], a[1]), rws, lane * 4, WS_PB + slot * 256, AUX_SC1);
      if (lane == 0) __builtin_amdgcn_raw_buffer_store_b64(u32x2{__float_as_uint(M), __float_as_uint(L)}, rws, 0, WS_MLB + slot * 16, AUX_SC1);
      WAIT_VM0();
      if (lane == 0) __builtin_amdgcn_raw_buffer_store_b64(tag, rws, 8, WS_MLB + slot * 16, AUX_SC1);
    }
    if (hw == 0) {
      unsigned mo = (lane & 3) * 16 + 8;
#pragma unroll 1
      for (unsigned spins = 0; spins < (1u << 17); ++spins) {
        __builtin_amdgcn_s_sleep(64);
        asm volatile("" : "+v"(mo) :: "memory");
        const u32x2 f = __builtin_amdgcn_raw_buffer_load_b64(rws, mo, WS_MLB + j * 64, AUX_SC1);
        if (__builtin_amdgcn_ballot_w64(f[0] != tag[0] || f[1] != tag[1]) == 0) break;
      }
      lds_signal(sync + S_ML_SEEN, lane);
    }
    lds_wait_ge(sync + S_ML_SEEN, 1);
    u32x4 outB;
    {
      u32x4 A[4]; f32x4 m4, l4;
#pragma unroll
      for (int s = 0; s < 4; ++s) A[s] = ld128<AUX_SC1>(rws, jj * 256 + g * 16 + s * 64, WS_PB + j * 1024);
#pragma unroll
      for (int t = 0; t < 4; ++t) { const u32x2 ml = __builtin_amdgcn_raw_buffer_load_b64(rws, t * 16, WS_MLB + j * 64, AUX_SC1); m4[t] = __uint_as_float(ml[0]); l4[t] = __uint_as_float(ml[1]); }
      outB = project4(A, T, m4, l4);
    }
    asm volatile("" : "+v"(outB));
    STAMP(5);
    SB();
#pragma unroll
    for (int i = 0; i < 32; ++i) T[i] = *(LAS u32x4*)(lds + L_WOA + hw * 32768 + i * 1024 + lane * 16);
#pragma unroll
    for (int i = 0; i < 32; ++i) asm volatile("" : "+v"(T[i]));
    STAMP(6);
    lds_wait_ge(sync + S_AP_CNT, 4);
    STAMP(7);
    {
      u32x4 A[4];
      const f32x4 m4 = *(LAS f32x4*)(lds + L_MLA), l4 = *(LAS f32x4*)(lds + L_MLA + 16);
#pragma unroll
      for (int s = 0; s < 4; ++s) A[s] = *(LAS u32x4*)(lds + L_PA + jj * 256 + g * 16 + s * 64);
      u32x4 o = project4(A, T, m4, l4);
      o = (g & 1) ? outB : o;
      asm volatile("" : "+v"(o));
      if (g < 2) st128(o, rws, op_off);
    }
    STAMP(8);
    if (hw == 0) {
      unsigned v[8];
#pragma unroll
      for (int i = 0; i < 8; ++i) v[i] = SENT;
      const unsigned vo = (unsigned)(8 * g) * 2048u + c * 4, base = WS_OUTP + b * 32 * 2048 + h * 64;
#pragma unroll 1
      for (unsigned spins = 0; spins < (1u << 18); ++spins) {
        unsigned o[8], t[8];
#pragma unroll
        for (int i = 0; i < 8; ++i) { o[i] = v[i] == SENT ? vo : 0x80000000u; asm volatile("" : "+v"(o[i]) :: "memory"); }
#pragma unroll
        for (int i = 0; i < 8; ++i) t[i] = __builtin_amdgcn_raw_buffer_load_b32(rws, o[i], base + i * 2048, AUX_SC1);
        bool ok = true;
#pragma unroll
        for (int i = 0; i < 8; ++i) { v[i] = v[i] == SENT ? t[i] : v[i]; ok &= v[i] != SENT; }
        if (__builtin_amdgcn_ballot_w64(!ok) == 0) break;
      }
      float s0 = 0.f, s1 = 0.f;
#pragma unroll
      for (int i = 0; i < 8; ++i) { s0 += __uint_as_float(v[i] << 16); s1 += __uint_as_float(v[i] & 0xffff0000u); }
      s0 = xrow_sum(s0); s1 = xrow_sum(s1);
      if (g == 0) *(unsigned*)(out + b * 1024 + (c >> 3) * 512 + h * 16 + (c & 7) * 2) = cvtpk(s0, s1);
      if (lane == 0) __builtin_amdgcn_raw_buffer_store_b64(u32x2{~tag[0], ~tag[1]}, rws, 0, WS_FLAG + j * 8, AUX_SC1);
    }
    STAMP(9);
  }
  STAMP_FLUSH();
}

extern "C" void kernel_launch(void* const* d_in, const int* in_sizes, int n_in, void* d_out, int out_size, void* d_ws, size_t ws_size, hipStream_t stream) {
  k_fused<<<256, 512, 0, stream>>>((const uint16_t*)d_in[0], (const uint16_t*)d_in[1], (const uint16_t*)d_in[2], (const uint16_t*)d_in[3], (const uint16_t*)d_in[4],
                                   (const uint16_t*)d_in[5], (const uint16_t*)d_in[6], (const uint16_t*)d_in[7], (const uint16_t*)d_in[8], (const uint16_t*)d_in[9],
                                   (uint16_t*)d_out, (unsigned char*)d_ws);
}
```

```cpp
#define DIAG 99
#define DIAG_ROLE 1
#define DIAG_SUB 0
#include <hip/hip_runtime.h>
#include <hip/hip_bf16.h>
#include <stdint.h>

typedef unsigned u32x4 __attribute__((ext_vector_type(4)));
typedef unsigned u32x2 __attribute__((ext_vector_type(2)));
typedef float f32x4 __attribute__((ext_vector_type(4)));
typedef float f32x2 __attribute__((ext_vector_type(2)));
typedef short s16x4 __attribute__((ext_vector_type(4)));
typedef __bf16 bf16x8 __attribute__((ext_vector_type(8)));
typedef __bf16 bf16x2 __attribute__((ext_vector_type(2)));
typedef __amdgpu_buffer_rsrc_t rsrc_t;
#define LAS __attribute__((address_space(3)))
#define SB() __builtin_amdgcn_sched_barrier(0)
#define AUX_NT 2
#define AUX_SC1 16
#define DEPTH 16
#define HPRIO 3
#define D0 16
#define QD 32
#define RATE 7.0
#define GG 4
#define QP 16
#define P2END 22
#define T1 20

constexpr int DH = 128, DM = 512;
constexpr float CSC = 0.088388347648318440f * 1.4426950408889634f;
constexpr float NEG_BIG = -3.4028234663852886e38f;
constexpr unsigned SENT = 0xFFFFFFFFu;

__device__ __forceinline__ rsrc_t mk(const void* p, unsigned bytes) { return __builtin_amdgcn_make_buffer_rsrc((void*)p, 0, bytes, 0x00020000); }
template <int AUX> __device__ __forceinline__ u32x4 ld128(rsrc_t r, unsigned voff, unsigned soff) { return __builtin_amdgcn_raw_buffer_load_b128(r, voff, soff, AUX); }
__device__ __forceinline__ void st128(u32x4 v, rsrc_t r, unsigned off) { __builtin_amdgcn_raw_buffer_store_b128(v, r, off, 0, AUX_SC1); }
__device__ __forceinline__ f32x4 mfma32(u32x4 a, u32x4 b, f32x4 c) {
  return __builtin_amdgcn_mfma_f32_16x16x32_bf16(__builtin_bit_cast(bf16x8, a), __builtin_bit_cast(bf16x8, b), c, 0, 0, 0);
}
__device__ __forceinline__ f32x4 mfma16(unsigned a0, unsigned a1, unsigned b0, unsigned b1, f32x4 c) {
  u32x2 a = {a0, a1}, b = {b0, b1};
  return __builtin_amdgcn_mfma_f32_16x16x16bf16_1k(__builtin_bit_cast(s16x4, a), __builtin_bit_cast(s16x4, b), c, 0, 0, 0);
}
__device__ __forceinline__ unsigned cvtpk(float lo, float hi) {
  f32x2 v = {lo, hi};
  return __builtin_bit_cast(unsigned, __builtin_convertvector(v, bf16x2));
}
template <int CTRL> __device__ __forceinline__ float dpp(float x) {
  return __builtin_bit_cast(float, __builtin_amdgcn_mov_dpp(__builtin_bit_cast(int, x), CTRL, 0xf, 0xf, true));
}
__device__ __forceinline__ float xrow_max(float x) {
  auto s = __builtin_amdgcn_permlane16_swap(__float_as_uint(x), __float_as_uint(x), false, false);
  x = fmaxf(__uint_as_float(s[0]), __uint_as_float(s[1]));
  auto t = __builtin_amdgcn_permlane32_swap(__float_as_uint(x), __float_as_uint(x), false, false);
  return fmaxf(__uint_as_float(t[0]), __uint_as_float(t[1]));
}
__device__ __forceinline__ float xrow_sum(float x) {
  auto s = __builtin_amdgcn_permlane16_swap(__float_as_uint(x), __float_as_uint(x), false, false);
  x = __uint_as_float(s[0]) + __uint_as_float(s[1]);
  auto t = __builtin_amdgcn_permlane32_swap(__float_as_uint(x), __float_as_uint(x), false, false);
  return __uint_as_float(t[0]) + __uint_as_float(t[1]);
}
__device__ __forceinline__ unsigned nosent(unsigned v) { return v < 0xFFFFFFFEu ? v : 0xFFFFFFFEu; }
#define WAIT_LGKM() asm volatile("s_waitcnt lgkmcnt(0)" ::: "memory")
#define WAIT_VM0() asm volatile("s_waitcnt vmcnt(0)" ::: "memory")
template <int N> __device__ __forceinline__ void waitvm() { asm volatile("s_waitcnt vmcnt(%0)" :: "n"(N)); }
__device__ __forceinline__ unsigned now32() { return (unsigned)__builtin_amdgcn_s_memrealtime(); }
__device__ __forceinline__ void pace_until(unsigned target) {
#pragma unroll 1
  for (int spins = 0; spins < 4096 && (int)(now32() - target) < 0; ++spins) __builtin_amdgcn_s_sleep(1);
}

constexpr unsigned TAU = (unsigned)(1.048576 / RATE * 100 * 256 + 0.5);
constexpr unsigned TAU1 = TAU + TAU * 4 / 32;
constexpr unsigned TAU2 = TAU + TAU * 12 / ((P2END - 2) * 16);
constexpr unsigned PACE = (P2END - 2) * 16 * TAU2 / 256 / 104;
struct Pacer {
  unsigned t0, acc, step;
  __device__ __forceinline__ void gate() {
    const unsigned target = t0 + (acc >> 8);
#pragma unroll 1
    for (int spins = 0; spins < 4096 && (int)(now32() - target) < 0; ++spins) __builtin_amdgcn_s_sleep(1);
    acc += step;
  }
};

constexpr unsigned WS_EPOCH = 0;
constexpr unsigned WS_FLAG = 2048;
constexpr unsigned WS_QB = 4096;
constexpr unsigned WS_PB = WS_QB + 256 * 256;
constexpr unsigned WS_MLB = WS_PB + 256 * 1024;
constexpr unsigned WS_OUTP = WS_MLB + 256 * 64;
constexpr unsigned WS_END = WS_OUTP + 256 * 2048;
#define STAMP(k) do { if ((k) == DIAG) dstamp = now32(); } while (0)
#define STAMP_DECL const unsigned tstart = now32(); unsigned dstamp = tstart + DIAG_SUB
#define STAMP_FLUSH() do { if ((wave >= 4) == DIAG_ROLE) { const unsigned tg = tstart + 15000 + 10 * (dstamp - tstart - DIAG_SUB); while ((int)(now32() - tg) < 0) __builtin_amdgcn_s_sleep(4); } } while (0)
constexpr int L_QPA = 0;
constexpr int L_QPB = L_QPA + 2048;
constexpr int L_QFB = L_QPB + 2048;
constexpr int L_PB = L_QFB + 1024;
constexpr int L_MLB = L_PB + 8192;
constexpr int L_PA = L_MLB + 128;
constexpr int L_MLA = L_PA + 1024;
constexpr int L_SYNC = L_MLA + 32;
constexpr int L_WOA = L_SYNC + 32;
constexpr int L_END = L_WOA + 131072;
enum { S_QB_CNT = 0, S_QB_READY = 1, S_BP_CNT = 2, S_AP_CNT = 3, S_ML_SEEN = 4, S_QA_CNT = 5 };

__device__ __forceinline__ void lds_wait_ge(LAS unsigned* w, unsigned target) {
#pragma unroll 1
  for (unsigned spins = 0; spins < (1u << 22); ++spins) {
    const unsigned v = __builtin_amdgcn_readfirstlane(__hip_atomic_load(w, __ATOMIC_RELAXED, __HIP_MEMORY_SCOPE_WORKGROUP));
    if (v >= target) break;
    __builtin_amdgcn_s_sleep(1);
  }
  asm volatile("" ::: "memory");
}
__device__ __forceinline__ void lds_signal(LAS unsigned* w, int lane) {
  WAIT_LGKM();
  if (lane == 0) __hip_atomic_fetch_add(w, 1u, __ATOMIC_RELAXED, __HIP_MEMORY_SCOPE_WORKGROUP);
}

constexpr int U = 8;
constexpr unsigned TILE = U * 1024;
struct State { float m, mc, l; f32x4 lo, hi; };
__device__ __forceinline__ void st_init(State& st) { st.m = NEG_BIG; st.mc = NEG_BIG * CSC; st.l = 0.f; st.lo = f32x4{0, 0, 0, 0}; st.hi = st.lo; }
__device__ __forceinline__ float quad_diag(const f32x4 d) { return ((dpp<0x00>(d[0]) + dpp<0x55>(d[1])) + dpp<0xAA>(d[2])) + dpp<0xFF>(d[3]); }

template <bool RELOAD>
__device__ __forceinline__ void roll(u32x4 (&K)[U], u32x4 (&V)[U], const u32x4 Qf, State& st, rsrc_t rk, rsrc_t rv, unsigned so,
                                     unsigned kv, unsigned vv, unsigned mask0, unsigned mask1, int lane, const unsigned (&lo)[U], Pacer& pc) {
  float s[U];
  f32x4 d[U];
  const f32x4 z = {0, 0, 0, 0};
#pragma unroll
  for (int i = 0; i < U; ++i) {
    d[i] = mfma32(K[i], Qf, z);
    if (RELOAD) {
      if (i % GG == 0) pc.gate();
      if (DEPTH < 2 * U) waitvm<DEPTH - 1>();
      K[i] = ld128<AUX_NT>(rk, kv, so + lo[i]);
    }
    if (i > 0) s[i - 1] = quad_diag(d[i - 1]);
    if (RELOAD) SB();
  }
  s[U - 1] = quad_diag(d[U - 1]);
  float mx = s[0];
#pragma unroll
  for (int i = 1; i < U; ++i) mx = fmaxf(mx, s[i]);
  if (__builtin_amdgcn_ballot_w64(mx > st.m) != 0) {
    const float mn = fmaxf(st.m, xrow_max(mx));
    const float alpha = __builtin_amdgcn_exp2f((st.m - mn) * CSC);
    st.l *= alpha; st.m = mn; st.mc = mn * CSC;
    const float a2 = __builtin_bit_cast(float, __builtin_amdgcn_ds_bpermute((lane >> 4) * 16, __builtin_bit_cast(int, alpha)));
    st.lo *= a2; st.hi *= a2;
  }
  SB();
#pragma unroll
  for (int i = 0; i < U; ++i) {
    const float p = __builtin_amdgcn_exp2f(fmaf(s[i], CSC, -st.mc));
    st.l += p;
    const unsigned t = cvtpk(p, p);
    const unsigned a0 = t & mask0, a1 = t & mask1;
    st.lo = mfma16(a0, a1, V[i][0], V[i][1], st.lo);
    st.hi = mfma16(a0, a1, V[i][2], V[i][3], st.hi);
    if (RELOAD) {
      if (i % GG == 0) pc.gate();
      if (DEPTH < 2 * U) waitvm<DEPTH - 1>();
      V[i] = ld128<AUX_NT>(rv, vv, so + lo[i]);
      SB();
    }
  }
  SB();
}
template <int D, bool BAR = false>
__device__ __forceinline__ void issue_tile(u32x4 (&K)[U], u32x4 (&V)[U], rsrc_t rk, rsrc_t rv, unsigned so, unsigned kv, unsigned vv, const unsigned (&lo)[U], Pacer& pc) {
#pragma unroll
  for (int i = 0; i < U; ++i) { if (i % GG == 0) pc.gate(); waitvm<D - 1>(); K[i] = ld128<AUX_NT>(rk, kv, so + lo[i]); SB(); if (BAR && i == GG - 1) { __builtin_amdgcn_s_barrier(); SB(); } }
#pragma unroll
  for (int i = 0; i < U; ++i) { if (i % GG == 0) pc.gate(); waitvm<D - 1>(); V[i] = ld128<AUX_NT>(rv, vv, so + lo[i]); SB(); }
}

template <int D, int PC> struct QProj {
  static constexpr int NL = 32;
  u32x4 xr[NL / 8], w[NL];
  __device__ __forceinline__ void issue(const uint16_t* x, const uint16_t* wq, int b, int h, int r0, int g, int c, unsigned tslot) {
    rsrc_t rx = mk(x + b * DM, DM * 2), rw = mk(wq + h * DH, (unsigned)(DM * 4096 * 2 - h * DH * 2));
#pragma unroll
    for (int t = 0; t < NL / 8; ++t) { xr[t] = ld128<0>(rx, (NL * g) * 2 + t * 16, r0 * 2); SB(); }
    const unsigned vo = (unsigned)(NL * g) * 8192u + c * 16;
#pragma unroll
    for (int i = 0; i < NL; ++i) {
      if (PC && (i & 1) == 0) pace_until(tslot + i * PC);
      if (D < NL) waitvm<(D < NL ? D - 1 : 0)>();
      w[i] = ld128<AUX_NT>(rw, vo, (r0 + i) * 8192); SB();
    }
  }
  __device__ __forceinline__ void compute(f32x4& lo, f32x4& hi, int c) {
    const int jj = c & 3;
    const unsigned selL0 = jj == 0 ? 0x0c0c0100u : jj == 1 ? 0x01000c0cu : 0x0c0c0c0cu, selL1 = jj == 2 ? 0x0c0c0100u : jj == 3 ? 0x01000c0cu : 0x0c0c0c0cu;
    const unsigned selH0 = selL0 | (jj < 2 ? 0x02020202u & (jj == 0 ? 0x0000ffffu : 0xffff0000u) : 0u);
    const unsigned selH1 = selL1 | (jj >= 2 ? 0x02020202u & (jj == 2 ? 0x0000ffffu : 0xffff0000u) : 0u);
#pragma unroll
    for (int i = 0; i < NL; ++i) {
      const unsigned xd = xr[i >> 3][(i >> 1) & 3];
      const unsigned a0 = __builtin_amdgcn_perm(0u, xd, (i & 1) ? selH0 : selL0), a1 = __builtin_amdgcn_perm(0u, xd, (i & 1) ? selH1 : selL1);
      lo = mfma16(a0, a1, w[i][0], w[i][1], lo); hi = mfma16(a0, a1, w[i][2], w[i][3], hi);
    }
  }
};

__device__ __forceinline__ void wo_issue(u32x4 (&W)[8], rsrc_t rw, unsigned vo, int s, unsigned tslot) {
#pragma unroll
  for (int i = 0; i < 8; ++i) {
    if ((i & 1) == 0) pace_until(tslot + i * PACE);
    W[i] = ld128<AUX_NT>(rw, vo + (i & 3) * 1024, (32 * s + (i & 4)) * 1024); SB();
  }
}
__device__ __forceinline__ u32x4 wo_frag(const u32x4 (&W)[8], int d) {
  u32x4 T;
#pragma unroll
  for (int t = 0; t < 4; ++t) T[t] = __builtin_amdgcn_perm(W[2 * t + 1][d >> 1], W[2 * t][d >> 1], (d & 1) ? 0x07060302u : 0x05040100u);
  return T;
}
__device__ __forceinline__ u32x4 project4(const u32x4 (&A)[4], const u32x4 (&T)[32], const f32x4 m4, const f32x4 l4) {
  const float M = fmaxf(fmaxf(m4[0], m4[1]), fmaxf(m4[2], m4[3]));
  f32x4 w4;
#pragma unroll
  for (int t = 0; t < 4; ++t) w4[t] = __builtin_amdgcn_exp2f(m4[t] - M);
  const float L = (w4[0] * l4[0] + w4[1] * l4[1]) + (w4[2] * l4[2] + w4[3] * l4[3]);
  w4 *= 1.0f / L;
  float o[8];
#pragma unroll
  for (int d = 0; d < 8; ++d) {
    f32x4 D = {0, 0, 0, 0};
#pragma unroll
    for (int s = 0; s < 4; ++s) D = mfma32(A[s], T[s * 8 + d], D);
    o[d] = (D[0] * w4[0] + D[1] * w4[1]) + (D[2] * w4[2] + D[3] * w4[3]);
  }
  return u32x4{nosent(cvtpk(o[0], o[1])), nosent(cvtpk(o[2], o[3])), nosent(cvtpk(o[4], o[5])), nosent(cvtpk(o[6], o[7]))};
}

__global__ void __launch_bounds__(512, 2) k_fused(const uint16_t* xA, const uint16_t* KA, const uint16_t* VA, const uint16_t* wqA, const uint16_t* woA,
                                                  const uint16_t* xB, const uint16_t* KB, const uint16_t* VB, const uint16_t* wqB, const uint16_t* woB,
                                                  uint16_t* out, unsigned char* ws) {
  __shared__ __attribute__((aligned(16))) unsigned char smem[L_END];
  LAS unsigned char* lds = (LAS unsigned char*)smem;
  LAS unsigned* sync = (LAS unsigned*)(lds + L_SYNC);
  const int lane = threadIdx.x & 63, wave = __builtin_amdgcn_readfirstlane(threadIdx.x >> 6);
  const int j = blockIdx.x, b = j >> 5, h = j & 31;
  const int g = lane >> 4, c = lane & 15, jj = c & 3;
  STAMP_DECL;
  STAMP(0);
  if (wave < 4) {
    constexpr unsigned AB = 1u << 20, BB = 1u << 18;
    const unsigned kv = ((lane >> 2) & 3) * 256 + (4 * g + (lane & 3)) * 16, vv = lane * 16;
    rsrc_t rkA = mk((const char*)KA + (size_t)j * AB, AB), rvA = mk((const char*)VA + (size_t)j * AB, AB);
    u32x4 K[U], V[U], K0[U], V0[U], K1[U], V1[U];
    unsigned lo[U];
#pragma unroll
    for (int i = 0; i < U; ++i) lo[i] = (4 * ((i + (j >> 3)) & 7) + wave) * 1024;
    STAMP(11);
    Pacer pc; pc.t0 = now32(); pc.acc = 0; pc.step = TAU1 * GG;
    unsigned so;
    issue_tile<D0, true>(K0, V0, rkA, rvA, 0, kv, vv, lo, pc);
    issue_tile<D0>(K1, V1, rkA, rvA, TILE * 4, kv, vv, lo, pc);
    pc.step = TAU2 * GG;
    issue_tile<D0>(K, V, rkA, rvA, TILE * 8, kv, vv, lo, pc);
    rsrc_t rkB = mk((const char*)KB + (size_t)j * BB, BB), rvB = mk((const char*)VB + (size_t)j * BB, BB);
    STAMP(5);
    lds_wait_ge(sync + S_QA_CNT, 4);
    u32x4 QA;
    {
      f32x4 ql[4], hi[4];
#pragma unroll
      for (int w = 0; w < 4; ++w) { ql[w] = *(LAS f32x4*)(lds + L_QPA + w * 512 + (4 * g + jj) * 32); hi[w] = *(LAS f32x4*)(lds + L_QPA + w * 512 + (4 * g + jj) * 32 + 16); }
      SB();
      const f32x4 l = (ql[0] + ql[1]) + (ql[2] + ql[3]), hh = (hi[0] + hi[1]) + (hi[2] + hi[3]);
      QA = u32x4{cvtpk(l[0], l[1]), cvtpk(l[2], l[3]), cvtpk(hh[0], hh[1]), cvtpk(hh[2], hh[3])};
    }
    STAMP(1);
    const unsigned mask0 = jj == 0 ? 0x0000ffffu : jj == 1 ? 0xffff0000u : 0u, mask1 = jj == 2 ? 0x0000ffffu : jj == 3 ? 0xffff0000u : 0u;
    State st, sv; st_init(st); st_init(sv);
    constexpr int NTA = AB / (TILE * 4), NTB = BB / (TILE * 4);
    u32x4 Qf = QA;
    roll<false>(K0, V0, Qf, st, rkA, rvA, 0, kv, vv, mask0, mask1, lane, lo, pc);
    STAMP(16);
    roll<true>(K, V, Qf, st, rkA, rvA, TILE * 12, kv, vv, mask0, mask1, lane, lo, pc);
    STAMP(17);
    roll<false>(K1, V1, Qf, st, rkA, rvA, 0, kv, vv, mask0, mask1, lane, lo, pc);
    STAMP(18);
#pragma unroll 1
    for (int p = 3; p < NTA + NTB; ++p) {
      const int n = p + 1;
      const bool nB = n >= T1 && n < T1 + NTB;
      rsrc_t rk = nB ? rkB : rkA, rv = nB ? rvB : rvA;
      pc.step = n < P2END ? TAU2 * GG : TAU * GG;
      if (n == NTA + NTB) { pc.step = 0; pc.acc = 0; }
      so = __builtin_amdgcn_readfirstlane((nB ? n - T1 : (n < T1 ? n : n - NTB)) * (TILE * 4));
      roll<true>(K, V, Qf, st, rk, rv, so, kv, vv, mask0, mask1, lane, lo, pc);
      STAMP(16 + p);
      if (n == T1) {
        sv = st; st_init(st);
        STAMP(3);
        lds_wait_ge(sync + S_QB_READY, 1);
        STAMP(4);
        Qf = *(LAS u32x4*)(lds + L_QFB + lane * 16);
      } else if (n == T1 + NTB) {
        const float lt = xrow_sum(st.l);
        *(LAS f32x4*)(lds + L_PB + wave * 2048 + lane * 32) = st.lo; *(LAS f32x4*)(lds + L_PB + wave * 2048 + lane * 32 + 16) = st.hi;
        if (g == 0 && jj == 0) { *(LAS float*)(lds + L_MLB + (c >> 2) * 32 + wave * 4) = st.mc; *(LAS float*)(lds + L_MLB + (c >> 2) * 32 + 16 + wave * 4) = lt; }
        lds_signal(sync + S_BP_CNT, lane);
        st = sv; Qf = QA;
      }
    }
    {
      const float lt = xrow_sum(st.l);
      if (g == 0) *(LAS u32x4*)(lds + L_PA + wave * 256 + c * 16) = u32x4{cvtpk(st.lo[0], st.lo[1]), cvtpk(st.lo[2], st.lo[3]), cvtpk(st.hi[0], st.hi[1]), cvtpk(st.hi[2], st.hi[3])};
      if (lane == 0) { *(LAS float*)(lds + L_MLA + wave * 4) = st.mc; *(LAS float*)(lds + L_MLA + 16 + wave * 4) = lt; }
      lds_signal(sync + S_AP_CNT, lane);
    }
    STAMP(2);
  } else {
    const int hw = wave - 4;
    __builtin_amdgcn_s_setprio(HPRIO);
    const unsigned th0 = now32();
    if (hw == 0 && lane < 8) sync[lane] = 0u;
    WAIT_LGKM();
    __builtin_amdgcn_s_barrier();
    SB();
    const unsigned tbase = th0 + 32 * TAU1 / 256 + 100;
    rsrc_t rws = mk(ws, WS_END);
    const unsigned op_off = WS_OUTP + j * 2048 + hw * 512 + (c >> 1) * 64 + (g & 1) * 32 + (c & 1) * 16;
    u32x2 ep;
    {
      QProj<QD, QP> qp;
      if (g < 2) st128(u32x4{SENT, SENT, SENT, SENT}, rws, op_off);
      ep = __builtin_amdgcn_raw_buffer_load_b64(rws, 0, WS_EPOCH + b * 256, AUX_SC1);
      SB();
      qp.issue(xA, wqA, b, h, 128 * hw, g, c, th0);
      f32x4 lo = {0, 0, 0, 0}, hi = {0, 0, 0, 0};
      qp.compute(lo, hi, c);
      if (g == 0) { *(LAS f32x4*)(lds + L_QPA + hw * 512 + c * 32) = lo; *(LAS f32x4*)(lds + L_QPA + hw * 512 + c * 32 + 16) = hi; }
    }
    lds_signal(sync + S_QA_CNT, lane);
    STAMP(10);
    {
      f32x4 lo = {0, 0, 0, 0}, hi = {0, 0, 0, 0};
      { QProj<32, PACE> qp; qp.issue(xB, wqB, b, h, 128 * hw, g, c, tbase); qp.compute(lo, hi, c); SB(); }
      if (g == 0) { *(LAS f32x4*)(lds + L_QPB + hw * 512 + c * 32) = lo; *(LAS f32x4*)(lds + L_QPB + hw * 512 + c * 32 + 16) = hi; }
    }
    WAIT_VM0();
    lds_signal(sync + S_QB_CNT, lane);
    const unsigned long long tag64 = (((unsigned long long)ep[1] << 32) | ep[0]) + 1ull;
    const u32x2 tag = {(unsigned)tag64, (unsigned)(tag64 >> 32)};
    if (hw == 0) {
      lds_wait_ge(sync + S_QB_CNT, 4);
      f32x2 s = {0, 0};
#pragma unroll
      for (int w = 0; w < 4; ++w) s += *(LAS f32x2*)(lds + L_QPB + w * 512 + lane * 8);
      __builtin_amdgcn_raw_buffer_store_b32(cvtpk(s[0], s[1]), rws, lane * 4, WS_QB + j * 256, AUX_SC1);
      WAIT_VM0();
      if (lane == 0) __builtin_amdgcn_raw_buffer_store_b64(tag, rws, 0, WS_FLAG + j * 8, AUX_SC1);
    }
    STAMP(1);
    const unsigned wvo = (unsigned)(8 * g) * 1024u + hw * 256 + c * 16;
    u32x4 T[32];
    {
      rsrc_t rwA = mk(woA + (size_t)h * DH * DM, DH * DM * 2), rwB = mk(woB + (size_t)h * DH * DM, DH * DM * 2);
      const unsigned ts = tbase + 38 * PACE;
      u32x4 W0[8], W1[8];
      wo_issue(W0, rwA, wvo, 0, ts);
#pragma unroll
      for (int s = 0; s < 8; s += 2) {
        if (s + 1 < 4) wo_issue(W1, rwA, wvo, s + 1, ts + (s + 1) * 8 * PACE); else wo_issue(W1, rwB, wvo, s + 1 - 4, ts + (s + 1) * 8 * PACE);
#pragma unroll
        for (int d = 0; d < 8; ++d) { if (s < 4) *(LAS u32x4*)(lds + L_WOA + hw * 32768 + (s * 8 + d) * 1024 + lane * 16) = wo_frag(W0, d); else T[(s - 4) * 8 + d] = wo_frag(W0, d); }
        SB();
        if (s + 2 < 4) wo_issue(W0, rwA, wvo, s + 2, ts + (s + 2) * 8 * PACE); else if (s + 2 < 8) wo_issue(W0, rwB, wvo, s + 2 - 4, ts + (s + 2) * 8 * PACE);
#pragma unroll
        for (int d = 0; d < 8; ++d) { if (s + 1 < 4) *(LAS u32x4*)(lds + L_WOA + hw * 32768 + ((s + 1) * 8 + d) * 1024 + lane * 16) = wo_frag(W1, d); else T[(s + 1 - 4) * 8 + d] = wo_frag(W1, d); }
        SB();
        if (s == 0 && hw == 1) {
          unsigned fo = (lane & 31) * 8;
#pragma unroll 1
          for (unsigned spins = 0; spins < (1u << 17); ++spins) {
            asm volatile("" : "+v"(fo) :: "memory");
            const u32x2 f = __builtin_amdgcn_raw_buffer_load_b64(rws, fo, WS_FLAG + b * 256, AUX_SC1);
            if (__builtin_amdgcn_ballot_w64(f[0] != tag[0] || f[1] != tag[1]) == 0) break;
            __builtin_amdgcn_s_sleep(64);
          }
          asm volatile("" ::: "memory");
          if (h == 0 && lane == 0) __builtin_amdgcn_raw_buffer_store_b64(tag, rws, 0, WS_EPOCH + b * 256, AUX_SC1);
          const u32x4 q = ld128<AUX_SC1>(rws, (c >> 2) * 256 + (4 * g + jj) * 16, WS_QB + (j & ~3) * 256);
          *(LAS u32x4*)(lds + L_QFB + lane * 16) = q;
          lds_signal(sync + S_QB_READY, lane);
          STAMP(3);
        }
      }
    }
    STAMP(2);
    lds_wait_ge(sync + S_BP_CNT, 4);
    STAMP(4);
    {
      const f32x4 m4 = *(LAS f32x4*)(lds + L_MLB + hw * 32), l4 = *(LAS f32x4*)(lds + L_MLB + hw * 32 + 16);
      const float M = fmaxf(fmaxf(m4[0], m4[1]), fmaxf(m4[2], m4[3]));
      f32x2 a = {0, 0}; float L = 0.f;
#pragma unroll
      for (int w = 0; w < 4; ++w) { const float e = __builtin_amdgcn_exp2f(m4[w] - M); L += e * l4[w]; a += e * *(LAS f32x2*)(lds + L_PB + w * 2048 + hw * 512 + lane * 8); }
      const unsigned slot = ((j & ~3) + hw) * 4 + (h & 3);
      __builtin_amdgcn_raw_buffer_store_b32(cvtpk(a[0], a[1]), rws, lane * 4, WS_PB + slot * 256, AUX_SC1);
      if (lane == 0) __builtin_amdgcn_raw_buffer_store_b64(u32x2{__float_as_uint(M), __float_as_uint(L)}, rws, 0, WS_MLB + slot * 16, AUX_SC1);
      WAIT_VM0();
      if (lane == 0) __builtin_amdgcn_raw_buffer_store_b64(tag, rws, 8, WS_MLB + slot * 16, AUX_SC1);
    }
    if (hw == 0) {
      unsigned mo = (lane & 3) * 16 + 8;
#pragma unroll 1
      for (unsigned spins = 0; spins < (1u << 17); ++spins) {
        __builtin_amdgcn_s_sleep(64);
        asm volatile("" : "+v"(mo) :: "memory");
        const u32x2 f = __builtin_amdgcn_raw_buffer_load_b64(rws, mo, WS_MLB + j * 64, AUX_SC1);
        if (__builtin_amdgcn_ballot_w64(f[0] != tag[0] || f[1] != tag[1]) == 0) break;
      }
      lds_signal(sync + S_ML_SEEN, lane);
    }
    lds_wait_ge(sync + S_ML_SEEN, 1);
    u32x4 outB;
    {
      u32x4 A[4]; f32x4 m4, l4;
#pragma unroll
      for (int s = 0; s < 4; ++s) A[s] = ld128<AUX_SC1>(rws, jj * 256 + g * 16 + s * 64, WS_PB + j * 1024);
#pragma unroll
      for (int t = 0; t < 4; ++t) { const u32x2 ml = __builtin_amdgcn_raw_buffer_load_b64(rws, t * 16, WS_MLB + j * 64, AUX_SC1); m4[t] = __uint_as_float(ml[0]); l4[t] = __uint_as_float(ml[1]); }
      outB = project4(A, T, m4, l4);
    }
    asm volatile("" : "+v"(outB));
    STAMP(5);
    SB();
#pragma unroll
    for (int i = 0; i < 32; ++i) T[i] = *(LAS u32x4*)(lds + L_WOA + hw * 32768 + i * 1024 + lane * 16);
#pragma unroll
    for (int i = 0; i < 32; ++i) asm volatile("" : "+v"(T[i]));
    STAMP(6);
    lds_wait_ge(sync + S_AP_CNT, 4);
    STAMP(7);
    {
      u32x4 A[4];
      const f32x4 m4 = *(LAS f32x4*)(lds + L_MLA), l4 = *(LAS f32x4*)(lds + L_MLA + 16);
#pragma unroll
      for (int s = 0; s < 4; ++s) A[s] = *(LAS u32x4*)(lds + L_PA + jj * 256 + g * 16 + s * 64);
      u32x4 o = project4(A, T, m4, l4);
      o = (g & 1) ? outB : o;
      asm volatile("" : "+v"(o));
      if (g < 2) st128(o, rws, op_off);
    }
    STAMP(8);
    if (hw == 0) {
      unsigned v[8];
#pragma unroll
      for (int i = 0; i < 8; ++i) v[i] = SENT;
      const unsigned vo = (unsigned)(8 * g) * 2048u + c * 4, base = WS_OUTP + b * 32 * 2048 + h * 64;
#pragma unroll 1
      for (unsigned spins = 0; spins < (1u << 18); ++spins) {
        unsigned o[8], t[8];
#pragma unroll
        for (int i = 0; i < 8; ++i) { o[i] = v[i] == SENT ? vo : 0x80000000u; asm volatile("" : "+v"(o[i]) :: "memory"); }
#pragma unroll
        for (int i = 0; i < 8; ++i) t[i] = __builtin_amdgcn_raw_buffer_load_b32(rws, o[i], base + i * 2048, AUX_SC1);
        bool ok = true;
#pragma unroll
        for (int i = 0; i < 8; ++i) { v[i] = v[i] == SENT ? t[i] : v[i]; ok &= v[i] != SENT; }
        if (__builtin_amdgcn_ballot_w64(!ok) == 0) break;
      }
      float s0 = 0.f, s1 = 0.f;
#pragma unroll
      for (int i = 0; i < 8; ++i) { s0 += __uint_as_float(v[i] << 16); s1 += __uint_as_float(v[i] & 0xffff0000u); }
      s0 = xrow_sum(s0); s1 = xrow_sum(s1);
      if (g == 0) *(unsigned*)(out + b * 1024 + (c >> 3) * 512 + h * 16 + (c & 7) * 2) = cvtpk(s0, s1);
      if (lane == 0) __builtin_amdgcn_raw_buffer_store_b64(u32x2{~tag[0], ~tag[1]}, rws, 0, WS_FLAG + j * 8, AUX_SC1);
    }
    STAMP(9);
  }
  STAMP_FLUSH();
}

extern "C" void kernel_launch(void* const* d_in, const int* in_sizes, int n_in, void* d_out, int out_size, void* d_ws, size_t ws_size, hipStream_t stream) {
  k_fused<<<256, 512, 0, stream>>>((const uint16_t*)d_in[0], (const uint16_t*)d_in[1], (const uint16_t*)d_in[2], (const uint16_t*)d_in[3], (const uint16_t*)d_in[4],
                                   (const uint16_t*)d_in[5], (const uint16_t*)d_in[6], (const uint16_t*)d_in[7], (const uint16_t*)d_in[8], (const uint16_t*)d_in[9],
                                   (uint16_t*)d_out, (unsigned char*)d_ws);
}
```

```cpp
#define DIAG 99
#define DIAG_ROLE 1
#define DIAG_SUB 0
#define DIAG_EXPR CTZ(KA)*200+CTZ(VA)*6400
#include <hip/hip_runtime.h>
#include <hip/hip_bf16.h>
#include <stdint.h>
#include <stdio.h>
#define CTZ(p) ((unsigned)min(31, (int)__builtin_ctzll((unsigned long long)(p))))

typedef unsigned u32x4 __attribute__((ext_vector_type(4)));
typedef unsigned u32x2 __attribute__((ext_vector_type(2)));
typedef float f32x4 __attribute__((ext_vector_type(4)));
typedef float f32x2 __attribute__((ext_vector_type(2)));
typedef short s16x4 __attribute__((ext_vector_type(4)));
typedef __bf16 bf16x8 __attribute__((ext_vector_type(8)));
typedef __bf16 bf16x2 __attribute__((ext_vector_type(2)));
typedef __amdgpu_buffer_rsrc_t rsrc_t;
#define LAS __attribute__((address_space(3)))
#define SB() __builtin_amdgcn_sched_barrier(0)
#define AUX_NT 2
#define AUX_SC1 16
#define DEPTH 16
#define HPRIO 3
#define D0 16
#define QD 32
#define RATE 7.0
#define GG 4
#define QP 16
#define P2END 22
#define T1 20

constexpr int DH = 128, DM = 512;
constexpr float CSC = 0.088388347648318440f * 1.4426950408889634f;
constexpr float NEG_BIG = -3.4028234663852886e38f;
constexpr unsigned SENT = 0xFFFFFFFFu;

__device__ __forceinline__ rsrc_t mk(const void* p, unsigned bytes) { return __builtin_amdgcn_make_buffer_rsrc((void*)p, 0, bytes, 0x00020000); }
template <int AUX> __device__ __forceinline__ u32x4 ld128(rsrc_t r, unsigned voff, unsigned soff) { return __builtin_amdgcn_raw_buffer_load_b128(r, voff, soff, AUX); }
__device__ __forceinline__ void st128(u32x4 v, rsrc_t r, unsigned off) { __builtin_amdgcn_raw_buffer_store_b128(v, r, off, 0, AUX_SC1); }
__device__ __forceinline__ f32x4 mfma32(u32x4 a, u32x4 b, f32x4 c) {
  return __builtin_amdgcn_mfma_f32_16x16x32_bf16(__builtin_bit_cast(bf16x8, a), __builtin_bit_cast(bf16x8, b), c, 0, 0, 0);
}
__device__ __forceinline__ f32x4 mfma16(unsigned a0, unsigned a1, unsigned b0, unsigned b1, f32x4 c) {
  u32x2 a = {a0, a1}, b = {b0, b1};
  return __builtin_amdgcn_mfma_f32_16x16x16bf16_1k(__builtin_bit_cast(s16x4, a), __builtin_bit_cast(s16x4, b), c, 0, 0, 0);
}
__device__ __forceinline__ unsigned cvtpk(float lo, float hi) {
  f32x2 v = {lo, hi};
  return __builtin_bit_cast(unsigned, __builtin_convertvector(v, bf16x2));
}
template <int CTRL> __device__ __forceinline__ float dpp(float x) {
  return __builtin_bit_cast(float, __builtin_amdgcn_mov_dpp(__builtin_bit_cast(int, x), CTRL, 0xf, 0xf, true));
}
__device__ __forceinline__ float xrow_max(float x) {
  auto s = __builtin_amdgcn_permlane16_swap(__float_as_uint(x), __float_as_uint(x), false, false);
  x = fmaxf(__uint_as_float(s[0]), __uint_as_float(s[1]));
  auto t = __builtin_amdgcn_permlane32_swap(__float_as_uint(x), __float_as_uint(x), false, false);
  return fmaxf(__uint_as_float(t[0]), __uint_as_float(t[1]));
}
__device__ __forceinline__ float xrow_sum(float x) {
  auto s = __builtin_amdgcn_permlane16_swap(__float_as_uint(x), __float_as_uint(x), false, false);
  x = __uint_as_float(s[0]) + __uint_as_float(s[1]);
  auto t = __builtin_amdgcn_permlane32_swap(__float_as_uint(x), __float_as_uint(x), false, false);
  return __uint_as_float(t[0]) + __uint_as_float(t[1]);
}
__device__ __forceinline__ unsigned nosent(unsigned v) { return v < 0xFFFFFFFEu ? v : 0xFFFFFFFEu; }
#define WAIT_LGKM() asm volatile("s_waitcnt lgkmcnt(0)" ::: "memory")
#define WAIT_VM0() asm volatile("s_waitcnt vmcnt(0)" ::: "memory")
template <int N> __device__ __forceinline__ void waitvm() { asm volatile("s_waitcnt vmcnt(%0)" :: "n"(N)); }
__device__ __forceinline__ unsigned now32() { return (unsigned)__builtin_amdgcn_s_memrealtime(); }
__device__ __forceinline__ void pace_until(unsigned target) {
#pragma unroll 1
  for (int spins = 0; spins < 4096 && (int)(now32() - target) < 0; ++spins) __builtin_amdgcn_s_sleep(1);
}

constexpr unsigned TAU = (unsigned)(1.048576 / RATE * 100 * 256 + 0.5);
constexpr unsigned TAU1 = TAU + TAU * 4 / 32;
constexpr unsigned TAU2 = TAU + TAU * 12 / ((P2END - 2) * 16);
constexpr unsigned PACE = (P2END - 2) * 16 * TAU2 / 256 / 104;
struct Pacer {
  unsigned t0, acc, step;
  __device__ __forceinline__ void gate() {
    const unsigned target = t0 + (acc >> 8);
#pragma unroll 1
    for (int spins = 0; spins < 4096 && (int)(now32() - target) < 0; ++spins) __builtin_amdgcn_s_sleep(1);
    acc += step;
  }
};

constexpr unsigned WS_EPOCH = 0;
constexpr unsigned WS_FLAG = 2048;
constexpr unsigned WS_QB = 4096;
constexpr unsigned WS_PB = WS_QB + 256 * 256;
constexpr unsigned WS_MLB = WS_PB + 256 * 1024;
constexpr unsigned WS_OUTP = WS_MLB + 256 * 64;
constexpr unsigned WS_END = WS_OUTP + 256 * 2048;
#define STAMP(k) do { if ((k) == DIAG) dstamp = now32(); } while (0)
#define STAMP_DECL const unsigned tstart = now32(); unsigned dstamp = tstart + DIAG_SUB
#define STAMP_FLUSH() do { if ((wave >= 4) == DIAG_ROLE) { const unsigned tg = tstart + 15000 + (DIAG_EXPR); while ((int)(now32() - tg) < 0) __builtin_amdgcn_s_sleep(4); } } while (0)
constexpr int L_QPA = 0;
constexpr int L_QPB = L_QPA + 2048;
constexpr int L_QFB = L_QPB + 2048;
constexpr int L_PB = L_QFB + 1024;
constexpr int L_MLB = L_PB + 8192;
constexpr int L_PA = L_MLB + 128;
constexpr int L_MLA = L_PA + 1024;
constexpr int L_SYNC = L_MLA + 32;
constexpr int L_WOA = L_SYNC + 32;
constexpr int L_END = L_WOA + 131072;
enum { S_QB_CNT = 0, S_QB_READY = 1, S_BP_CNT = 2, S_AP_CNT = 3, S_ML_SEEN = 4, S_QA_CNT = 5 };

__device__ __forceinline__ void lds_wait_ge(LAS unsigned* w, unsigned target) {
#pragma unroll 1
  for (unsigned spins = 0; spins < (1u << 22); ++spins) {
    const unsigned v = __builtin_amdgcn_readfirstlane(__hip_atomic_load(w, __ATOMIC_RELAXED, __HIP_MEMORY_SCOPE_WORKGROUP));
    if (v >= target) break;
    __builtin_amdgcn_s_sleep(1);
  }
  asm volatile("" ::: "memory");
}
__device__ __forceinline__ void lds_signal(LAS unsigned* w, int lane) {
  WAIT_LGKM();
  if (lane == 0) __hip_atomic_fetch_add(w, 1u, __ATOMIC_RELAXED, __HIP_MEMORY_SCOPE_WORKGROUP);
}

constexpr int U = 8;
constexpr unsigned TILE = U * 1024;
struct State { float m, mc, l; f32x4 lo, hi; };
__device__ __forceinline__ void st_init(State& st) { st.m = NEG_BIG; st.mc = NEG_BIG * CSC; st.l = 0.f; st.lo = f32x4{0, 0, 0, 0}; st.hi = st.lo; }
__device__ __forceinline__ float quad_diag(const f32x4 d) { return ((dpp<0x00>(d[0]) + dpp<0x55>(d[1])) + dpp<0xAA>(d[2])) + dpp<0xFF>(d[3]); }

template <bool RELOAD>
__device__ __forceinline__ void roll(u32x4 (&K)[U], u32x4 (&V)[U], const u32x4 Qf, State& st, rsrc_t rk, rsrc_t rv, unsigned so,
                                     unsigned kv, unsigned vv, unsigned mask0, unsigned mask1, int lane, const unsigned (&lo)[U], Pacer& pc) {
  float s[U];
  f32x4 d[U];
  const f32x4 z = {0, 0, 0, 0};
#pragma unroll
  for (int i = 0; i < U; ++i) {
    d[i] = mfma32(K[i], Qf, z);
    if (RELOAD) {
      if (i % GG == 0) pc.gate();
      if (DEPTH < 2 * U) waitvm<DEPTH - 1>();
      K[i] = ld128<AUX_NT>(rk, kv, so + lo[i]);
    }
    if (i > 0) s[i - 1] = quad_diag(d[i - 1]);
    if (RELOAD) SB();
  }
  s[U - 1] = quad_diag(d[U - 1]);
  float mx = s[0];
#pragma unroll
  for (int i = 1; i < U; ++i) mx = fmaxf(mx, s[i]);
  if (__builtin_amdgcn_ballot_w64(mx > st.m) != 0) {
    const float mn = fmaxf(st.m, xrow_max(mx));
    const float alpha = __builtin_amdgcn_exp2f((st.m - mn) * CSC);
    st.l *= alpha; st.m = mn; st.mc = mn * CSC;
    const float a2 = __builtin_bit_cast(float, __builtin_amdgcn_ds_bpermute((lane >> 4) * 16, __builtin_bit_cast(int, alpha)));
    st.lo *= a2; st.hi *= a2;
  }
  SB();
#pragma unroll
  for (int i = 0; i < U; ++i) {
    const float p = __builtin_amdgcn_exp2f(fmaf(s[i], CSC, -st.mc));
    st.l += p;
    const unsigned t = cvtpk(p, p);
    const unsigned a0 = t & mask0, a1 = t & mask1;
    st.lo = mfma16(a0, a1, V[i][0], V[i][1], st.lo);
    st.hi = mfma16(a0, a1, V[i][2], V[i][3], st.hi);
    if (RELOAD) {
      if (i % GG == 0) pc.gate();
      if (DEPTH < 2 * U) waitvm<DEPTH - 1>();
      V[i] = ld128<AUX_NT>(rv, vv, so + lo[i]);
      SB();
    }
  }
  SB();
}
template <int D, bool BAR = false>
__device__ __forceinline__ void issue_tile(u32x4 (&K)[U], u32x4 (&V)[U], rsrc_t rk, rsrc_t rv, unsigned so, unsigned kv, unsigned vv, const unsigned (&lo)[U], Pacer& pc) {
#pragma unroll
  for (int i = 0; i < U; ++i) { if (i % GG == 0) pc.gate(); waitvm<D - 1>(); K[i] = ld128<AUX_NT>(rk, kv, so + lo[i]); SB(); if (BAR && i == GG - 1) { __builtin_amdgcn_s_barrier(); SB(); } }
#pragma unroll
  for (int i = 0; i < U; ++i) { if (i % GG == 0) pc.gate(); waitvm<D - 1>(); V[i] = ld128<AUX_NT>(rv, vv, so + lo[i]); SB(); }
}

template <int D, int PC> struct QProj {
  static constexpr int NL = 32;
  u32x4 xr[NL / 8], w[NL];
  __device__ __forceinline__ void issue(const uint16_t* x, const uint16_t* wq, int b, int h, int r0, int g, int c, unsigned tslot) {
    rsrc_t rx = mk(x + b * DM, DM * 2), rw = mk(wq + h * DH, (unsigned)(DM * 4096 * 2 - h * DH * 2));
#pragma unroll
    for (int t = 0; t < NL / 8; ++t) { xr[t] = ld128<0>(rx, (NL * g) * 2 + t * 16, r0 * 2); SB(); }
    const unsigned vo = (unsigned)(NL * g) * 8192u + c * 16;
#pragma unroll
    for (int i = 0; i < NL; ++i) {
      if (PC && (i & 1) == 0) pace_until(tslot + i * PC);
      if (D < NL) waitvm<(D < NL ? D - 1 : 0)>();
      w[i] = ld128<AUX_NT>(rw, vo, (r0 + i) * 8192); SB();
    }
  }
  __device__ __forceinline__ void compute(f32x4& lo, f32x4& hi, int c) {
    const int jj = c & 3;
    const unsigned selL0 = jj == 0 ? 0x0c0c0100u : jj == 1 ? 0x01000c0cu : 0x0c0c0c0cu, selL1 = jj == 2 ? 0x0c0c0100u : jj == 3 ? 0x01000c0cu : 0x0c0c0c0cu;
    const unsigned selH0 = selL0 | (jj < 2 ? 0x02020202u & (jj == 0 ? 0x0000ffffu : 0xffff0000u) : 0u);
    const unsigned selH1 = selL1 | (jj >= 2 ? 0x02020202u & (jj == 2 ? 0x0000ffffu : 0xffff0000u) : 0u);
#pragma unroll
    for (int i = 0; i < NL; ++i) {
      const unsigned xd = xr[i >> 3][(i >> 1) & 3];
      const unsigned a0 = __builtin_amdgcn_perm(0u, xd, (i & 1) ? selH0 : selL0), a1 = __builtin_amdgcn_perm(0u, xd, (i & 1) ? selH1 : selL1);
      lo = mfma16(a0, a1, w[i][0], w[i][1], lo); hi = mfma16(a0, a1, w[i][2], w[i][3], hi);
    }
  }
};

__device__ __forceinline__ void wo_issue(u32x4 (&W)[8], rsrc_t rw, unsigned vo, int s, unsigned tslot) {
#pragma unroll
  for (int i = 0; i < 8; ++i) {
    if ((i & 1) == 0) pace_until(tslot + i * PACE);
    W[i] = ld128<AUX_NT>(rw, vo + (i & 3) * 1024, (32 * s + (i & 4)) * 1024); SB();
  }
}
__device__ __forceinline__ u32x4 wo_frag(const u32x4 (&W)[8], int d) {
  u32x4 T;
#pragma unroll
  for (int t = 0; t < 4; ++t) T[t] = __builtin_amdgcn_perm(W[2 * t + 1][d >> 1], W[2 * t][d >> 1], (d & 1) ? 0x07060302u : 0x05040100u);
  return T;
}
__device__ __forceinline__ u32x4 project4(const u32x4 (&A)[4], const u32x4 (&T)[32], const f32x4 m4, const f32x4 l4) {
  const float M = fmaxf(fmaxf(m4[0], m4[1]), fmaxf(m4[2], m4[3]));
  f32x4 w4;
#pragma unroll
  for (int t = 0; t < 4; ++t) w4[t] = __builtin_amdgcn_exp2f(m4[t] - M);
  const float L = (w4[0] * l4[0] + w4[1] * l4[1]) + (w4[2] * l4[2] + w4[3] * l4[3]);
  w4 *= 1.0f / L;
  float o[8];
#pragma unroll
  for (int d = 0; d < 8; ++d) {
    f32x4 D = {0, 0, 0, 0};
#pragma unroll
    for (int s = 0; s < 4; ++s) D = mfma32(A[s], T[s * 8 + d], D);
    o[d] = (D[0] * w4[0] + D[1] * w4[1]) + (D[2] * w4[2] + D[3] * w4[3]);
  }
  return u32x4{nosent(cvtpk(o[0], o[1])), nosent(cvtpk(o[2], o[3])), nosent(cvtpk(o[4], o[5])), nosent(cvtpk(o[6], o[7]))};
}

__global__ void __launch_bounds__(512, 2) k_fused(const uint16_t* xA, const uint16_t* KA, const uint16_t* VA, const uint16_t* wqA, const uint16_t* woA,
                                                  const uint16_t* xB, const uint16_t* KB, const uint16_t* VB, const uint16_t* wqB, const uint16_t* woB,
                                                  uint16_t* out, unsigned char* ws) {
  __shared__ __attribute__((aligned(16))) unsigned char smem[L_END];
  LAS unsigned char* lds = (LAS unsigned char*)smem;
  LAS unsigned* sync = (LAS unsigned*)(lds + L_SYNC);
  const int lane = threadIdx.x & 63, wave = __builtin_amdgcn_readfirstlane(threadIdx.x >> 6);
  const int j = blockIdx.x, b = j >> 5, h = j & 31;
  const int g = lane >> 4, c = lane & 15, jj = c & 3;
  STAMP_DECL;
  STAMP(0);
  if (wave < 4) {
    constexpr unsigned AB = 1u << 20, BB = 1u << 18;
    const unsigned kv = ((lane >> 2) & 3) * 256 + (4 * g + (lane & 3)) * 16, vv = lane * 16;
    rsrc_t rkA = mk((const char*)KA + (size_t)j * AB, AB), rvA = mk((const char*)VA + (size_t)j * AB, AB);
    u32x4 K[U], V[U], K0[U], V0[U], K1[U], V1[U];
    unsigned lo[U];
#pragma unroll
    for (int i = 0; i < U; ++i) lo[i] = (4 * ((i + (j >> 3)) & 7) + wave) * 1024;
    STAMP(11);
    Pacer pc; pc.t0 = now32(); pc.acc = 0; pc.step = TAU1 * GG;
    unsigned so;
    issue_tile<D0, true>(K0, V0, rkA, rvA, 0, kv, vv, lo, pc);
    issue_tile<D0>(K1, V1, rkA, rvA, TILE * 4, kv, vv, lo, pc);
    pc.step = TAU2 * GG;
    issue_tile<D0>(K, V, rkA, rvA, TILE * 8, kv, vv, lo, pc);
    rsrc_t rkB = mk((const char*)KB + (size_t)j * BB, BB), rvB = mk((const char*)VB + (size_t)j * BB, BB);
    STAMP(5);
    lds_wait_ge(sync + S_QA_CNT, 4);
    u32x4 QA;
    {
      f32x4 ql[4], hi[4];
#pragma unroll
      for (int w = 0; w < 4; ++w) { ql[w] = *(LAS f32x4*)(lds + L_QPA + w * 512 + (4 * g + jj) * 32); hi[w] = *(LAS f32x4*)(lds + L_QPA + w * 512 + (4 * g + jj) * 32 + 16); }
      SB();
      const f32x4 l = (ql[0] + ql[1]) + (ql[2] + ql[3]), hh = (hi[0] + hi[1]) + (hi[2] + hi[3]);
      QA = u32x4{cvtpk(l[0], l[1]), cvtpk(l[2], l[3]), cvtpk(hh[0], hh[1]), cvtpk(hh[2], hh[3])};
    }
    STAMP(1);
    const unsigned mask0 = jj == 0 ? 0x0000ffffu : jj == 1 ? 0xffff0000u : 0u, mask1 = jj == 2 ? 0x0000ffffu : jj == 3 ? 0xffff0000u : 0u;
    State st, sv; st_init(st); st_init(sv);
    constexpr int NTA = AB / (TILE * 4), NTB = BB / (TILE * 4);
    u32x4 Qf = QA;
    roll<false>(K0, V0, Qf, st, rkA, rvA, 0, kv, vv, mask0, mask1, lane, lo, pc);
    STAMP(16);
    roll<true>(K, V, Qf, st, rkA, rvA, TILE * 12, kv, vv, mask0, mask1, lane, lo, pc);
    STAMP(17);
    roll<false>(K1, V1, Qf, st, rkA, rvA, 0, kv, vv, mask0, mask1, lane, lo, pc);
    STAMP(18);
#pragma unroll 1
    for (int p = 3; p < NTA + NTB; ++p) {
      const int n = p + 1;
      const bool nB = n >= T1 && n < T1 + NTB;
      rsrc_t rk = nB ? rkB : rkA, rv = nB ? rvB : rvA;
      pc.step = n < P2END ? TAU2 * GG : TAU * GG;
      if (n == NTA + NTB) { pc.step = 0; pc.acc = 0; }
      so = __builtin_amdgcn_readfirstlane((nB ? n - T1 : (n < T1 ? n : n - NTB)) * (TILE * 4));
      roll<true>(K, V, Qf, st, rk, rv, so, kv, vv, mask0, mask1, lane, lo, pc);
      STAMP(16 + p);
      if (n == T1) {
        sv = st; st_init(st);
        STAMP(3);
        lds_wait_ge(sync + S_QB_READY, 1);
        STAMP(4);
        Qf = *(LAS u32x4*)(lds + L_QFB + lane * 16);
      } else if (n == T1 + NTB) {
        const float lt = xrow_sum(st.l);
        *(LAS f32x4*)(lds + L_PB + wave * 2048 + lane * 32) = st.lo; *(LAS f32x4*)(lds + L_PB + wave * 2048 + lane * 32 + 16) = st.hi;
        if (g == 0 && jj == 0) { *(LAS float*)(lds + L_MLB + (c >> 2) * 32 + wave * 4) = st.mc; *(LAS float*)(lds + L_MLB + (c >> 2) * 32 + 16 + wave * 4) = lt; }
        lds_signal(sync + S_BP_CNT, lane);
        st = sv; Qf = QA;
      }
    }
    {
      const float lt = xrow_sum(st.l);
      if (g == 0) *(LAS u32x4*)(lds + L_PA + wave * 256 + c * 16) = u32x4{cvtpk(st.lo[0], st.lo[1]), cvtpk(st.lo[2], st.lo[3]), cvtpk(st.hi[0], st.hi[1]), cvtpk(st.hi[2], st.hi[3])};
      if (lane == 0) { *(LAS float*)(lds + L_MLA + wave * 4) = st.mc; *(LAS float*)(lds + L_MLA + 16 + wave * 4) = lt; }
      lds_signal(sync + S_AP_CNT, lane);
    }
    STAMP(2);
  } else {
    const int hw = wave - 4;
    __builtin_amdgcn_s_setprio(HPRIO);
    const unsigned th0 = now32();
    if (hw == 0 && lane < 8) sync[lane] = 0u;
    WAIT_LGKM();
    __builtin_amdgcn_s_barrier();
    SB();
    const unsigned tbase = th0 + 32 * TAU1 / 256 + 100;
    rsrc_t rws = mk(ws, WS_END);
    const unsigned op_off = WS_OUTP + j * 2048 + hw * 512 + (c >> 1) * 64 + (g & 1) * 32 + (c & 1) * 16;
    u32x2 ep;
    {
      QProj<QD, QP> qp;
      if (g < 2) st128(u32x4{SENT, SENT, SENT, SENT}, rws, op_off);
      ep = __builtin_amdgcn_raw_buffer_load_b64(rws, 0, WS_EPOCH + b * 256, AUX_SC1);
      SB();
      qp.issue(xA, wqA, b, h, 128 * hw, g, c, th0);
      f32x4 lo = {0, 0, 0, 0}, hi = {0, 0, 0, 0};
      qp.compute(lo, hi, c);
      if (g == 0) { *(LAS f32x4*)(lds + L_QPA + hw * 512 + c * 32) = lo; *(LAS f32x4*)(lds + L_QPA + hw * 512 + c * 32 + 16) = hi; }
    }
    lds_signal(sync + S_QA_CNT, lane);
    STAMP(10);
    {
      f32x4 lo = {0, 0, 0, 0}, hi = {0, 0, 0, 0};
      { QProj<32, PACE> qp; qp.issue(xB, wqB, b, h, 128 * hw, g, c, tbase); qp.compute(lo, hi, c); SB(); }
      if (g == 0) { *(LAS f32x4*)(lds + L_QPB + hw * 512 + c * 32) = lo; *(LAS f32x4*)(lds + L_QPB + hw * 512 + c * 32 + 16) = hi; }
    }
    WAIT_VM0();
    lds_signal(sync + S_QB_CNT, lane);
    const unsigned long long tag64 = (((unsigned long long)ep[1] << 32) | ep[0]) + 1ull;
    const u32x2 tag = {(unsigned)tag64, (unsigned)(tag64 >> 32)};
    if (hw == 0) {
      lds_wait_ge(sync + S_QB_CNT, 4);
      f32x2 s = {0, 0};
#pragma unroll
      for (int w = 0; w < 4; ++w) s += *(LAS f32x2*)(lds + L_QPB + w * 512 + lane * 8);
      __builtin_amdgcn_raw_buffer_store_b32(cvtpk(s[0], s[1]), rws, lane * 4, WS_QB + j * 256, AUX_SC1);
      WAIT_VM0();
      if (lane == 0) __builtin_amdgcn_raw_buffer_store_b64(tag, rws, 0, WS_FLAG + j * 8, AUX_SC1);
    }
    STAMP(1);
    const unsigned wvo = (unsigned)(8 * g) * 1024u + hw * 256 + c * 16;
    u32x4 T[32];
    {
      rsrc_t rwA = mk(woA + (size_t)h * DH * DM, DH * DM * 2), rwB = mk(woB + (size_t)h * DH * DM, DH * DM * 2);
      const unsigned ts = tbase + 38 * PACE;
      u32x4 W0[8], W1[8];
      wo_issue(W0, rwA, wvo, 0, ts);
#pragma unroll
      for (int s = 0; s < 8; s += 2) {
        if (s + 1 < 4) wo_issue(W1, rwA, wvo, s + 1, ts + (s + 1) * 8 * PACE); else wo_issue(W1, rwB, wvo, s + 1 - 4, ts + (s + 1) * 8 * PACE);
#pragma unroll
        for (int d = 0; d < 8; ++d) { if (s < 4) *(LAS u32x4*)(lds + L_WOA + hw * 32768 + (s * 8 + d) * 1024 + lane * 16) = wo_frag(W0, d); else T[(s - 4) * 8 + d] = wo_frag(W0, d); }
        SB();
        if (s + 2 < 4) wo_issue(W0, rwA, wvo, s + 2, ts + (s + 2) * 8 * PACE); else if (s + 2 < 8) wo_issue(W0, rwB, wvo, s + 2 - 4, ts + (s + 2) * 8 * PACE);
#pragma unroll
        for (int d = 0; d < 8; ++d) { if (s + 1 < 4) *(LAS u32x4*)(lds + L_WOA + hw * 32768 + ((s + 1) * 8 + d) * 1024 + lane * 16) = wo_frag(W1, d); else T[(s + 1 - 4) * 8 + d] = wo_frag(W1, d); }
        SB();
        if (s == 0 && hw == 1) {
          unsigned fo = (lane & 31) * 8;
#pragma unroll 1
          for (unsigned spins = 0; spins < (1u << 17); ++spins) {
            asm volatile("" : "+v"(fo) :: "memory");
            const u32x2 f = __builtin_amdgcn_raw_buffer_load_b64(rws, fo, WS_FLAG + b * 256, AUX_SC1);
            if (__builtin_amdgcn_ballot_w64(f[0] != tag[0] || f[1] != tag[1]) == 0) break;
            __builtin_amdgcn_s_sleep(64);
          }
          asm volatile("" ::: "memory");
          if (h == 0 && lane == 0) __builtin_amdgcn_raw_buffer_store_b64(tag, rws, 0, WS_EPOCH + b * 256, AUX_SC1);
          const u32x4 q = ld128<AUX_SC1>(rws, (c >> 2) * 256 + (4 * g + jj) * 16, WS_QB + (j & ~3) * 256);
          *(LAS u32x4*)(lds + L_QFB + lane * 16) = q;
          lds_signal(sync + S_QB_READY, lane);
          STAMP(3);
        }
      }
    }
    STAMP(2);
    lds_wait_ge(sync + S_BP_CNT, 4);
    STAMP(4);
    {
      const f32x4 m4 = *(LAS f32x4*)(lds + L_MLB + hw * 32), l4 = *(LAS f32x4*)(lds + L_MLB + hw * 32 + 16);
      const float M = fmaxf(fmaxf(m4[0], m4[1]), fmaxf(m4[2], m4[3]));
      f32x2 a = {0, 0}; float L = 0.f;
#pragma unroll
      for (int w = 0; w < 4; ++w) { const float e = __builtin_amdgcn_exp2f(m4[w] - M); L += e * l4[w]; a += e * *(LAS f32x2*)(lds + L_PB + w * 2048 + hw * 512 + lane * 8); }
      const unsigned slot = ((j & ~3) + hw) * 4 + (h & 3);
      __builtin_amdgcn_raw_buffer_store_b32(cvtpk(a[0], a[1]), rws, lane * 4, WS_PB + slot * 256, AUX_SC1);
      if (lane == 0) __builtin_amdgcn_raw_buffer_store_b64(u32x2{__float_as_uint(M), __float_as_uint(L)}, rws, 0, WS_MLB + slot * 16, AUX_SC1);
      WAIT_VM0();
      if (lane == 0) __builtin_amdgcn_raw_buffer_store_b64(tag, rws, 8, WS_MLB + slot * 16, AUX_SC1);
    }
    if (hw == 0) {
      unsigned mo = (lane & 3) * 16 + 8;
#pragma unroll 1
      for (unsigned spins = 0; spins < (1u << 17); ++spins) {
        __builtin_amdgcn_s_sleep(64);
        asm volatile("" : "+v"(mo) :: "memory");
        const u32x2 f = __builtin_amdgcn_raw_buffer_load_b64(rws, mo, WS_MLB + j * 64, AUX_SC1);
        if (__builtin_amdgcn_ballot_w64(f[0] != tag[0] || f[1] != tag[1]) == 0) break;
      }
      lds_signal(sync + S_ML_SEEN, lane);
    }
    lds_wait_ge(sync + S_ML_SEEN, 1);
    u32x4 outB;
    {
      u32x4 A[4]; f32x4 m4, l4;
#pragma unroll
      for (int s = 0; s < 4; ++s) A[s] = ld128<AUX_SC1>(rws, jj * 256 + g * 16 + s * 64, WS_PB + j * 1024);
#pragma unroll
      for (int t = 0; t < 4; ++t) { const u32x2 ml = __builtin_amdgcn_raw_buffer_load_b64(rws, t * 16, WS_MLB + j * 64, AUX_SC1); m4[t] = __uint_as_float(ml[0]); l4[t] = __uint_as_float(ml[1]); }
      outB = project4(A, T, m4, l4);
    }
    asm volatile("" : "+v"(outB));
    STAMP(5);
    SB();
#pragma unroll
    for (int i = 0; i < 32; ++i) T[i] = *(LAS u32x4*)(lds + L_WOA + hw * 32768 + i * 1024 + lane * 16);
#pragma unroll
    for (int i = 0; i < 32; ++i) asm volatile("" : "+v"(T[i]));
    STAMP(6);
    lds_wait_ge(sync + S_AP_CNT, 4);
    STAMP(7);
    {
      u32x4 A[4];
      const f32x4 m4 = *(LAS f32x4*)(lds + L_MLA), l4 = *(LAS f32x4*)(lds + L_MLA + 16);
#pragma unroll
      for (int s = 0; s < 4; ++s) A[s] = *(LAS u32x4*)(lds + L_PA + jj * 256 + g * 16 + s * 64);
      u32x4 o = project4(A, T, m4, l4);
      o = (g & 1) ? outB : o;
      asm volatile("" : "+v"(o));
      if (g < 2) st128(o, rws, op_off);
    }
    STAMP(8);
    if (hw == 0) {
      unsigned v[8];
#pragma unroll
      for (int i = 0; i < 8; ++i) v[i] = SENT;
      const unsigned vo = (unsigned)(8 * g) * 2048u + c * 4, base = WS_OUTP + b * 32 * 2048 + h * 64;
#pragma unroll 1
      for (unsigned spins = 0; spins < (1u << 18); ++spins) {
        unsigned o[8], t[8];
#pragma unroll
        for (int i = 0; i < 8; ++i) { o[i] = v[i] == SENT ? vo : 0x80000000u; asm volatile("" : "+v"(o[i]) :: "memory"); }
#pragma unroll
        for (int i = 0; i < 8; ++i) t[i] = __builtin_amdgcn_raw_buffer_load_b32(rws, o[i], base + i * 2048, AUX_SC1);
        bool ok = true;
#pragma unroll
        for (int i = 0; i < 8; ++i) { v[i] = v[i] == SENT ? t[i] : v[i]; ok &= v[i] != SENT; }
        if (__builtin_amdgcn_ballot_w64(!ok) == 0) break;
      }
      float s0 = 0.f, s1 = 0.f;
#pragma unroll
      for (int i = 0; i < 8; ++i) { s0 += __uint_as_float(v[i] << 16); s1 += __uint_as_float(v[i] & 0xffff0000u); }
      s0 = xrow_sum(s0); s1 = xrow_sum(s1);
      if (g == 0) *(unsigned*)(out + b * 1024 + (c >> 3) * 512 + h * 16 + (c & 7) * 2) = cvtpk(s0, s1);
      if (lane == 0) __builtin_amdgcn_raw_buffer_store_b64(u32x2{~tag[0], ~tag[1]}, rws, 0, WS_FLAG + j * 8, AUX_SC1);
    }
    STAMP(9);
  }
  STAMP_FLUSH();
}

extern "C" void kernel_launch(void* const* d_in, const int* in_sizes, int n_in, void* d_out, int out_size, void* d_ws, size_t ws_size, hipStream_t stream) {
  k_fused<<<256, 512, 0, stream>>>((const uint16_t*)d_in[0], (const uint16_t*)d_in[1], (const uint16_t*)d_in[2], (const uint16_t*)d_in[3], (const uint16_t*)d_in[4],
                                   (const uint16_t*)d_in[5], (const uint16_t*)d_in[6], (const uint16_t*)d_in[7], (const uint16_t*)d_in[8], (const uint16_t*)d_in[9],
                                   (uint16_t*)d_out, (unsigned char*)d_ws);
  for (int i = 0; i < 10; ++i) fprintf(stderr, "in%d %p\n", i, d_in[i]);
  fprintf(stderr, "out %p ws %p\n", d_out, d_ws);
}
```

```cpp
#define DIAG 13
#define DIAG_ROLE 0
#define DIAG_SUB 0
#define DIAG_SGN 1
#include <hip/hip_runtime.h>
#include <stdint.h>

typedef unsigned u32x4 __attribute__((ext_vector_type(4)));
typedef unsigned u32x2 __attribute__((ext_vector_type(2)));
typedef float f32x4 __attribute__((ext_vector_type(4)));
typedef float f32x2 __attribute__((ext_vector_type(2)));
typedef short s16x4 __attribute__((ext_vector_type(4)));
typedef __bf16 bf16x8 __attribute__((ext_vector_type(8)));
typedef __bf16 bf16x2 __attribute__((ext_vector_type(2)));
typedef __amdgpu_buffer_rsrc_t rsrc_t;
#define LAS __attribute__((address_space(3)))
#define SB() __builtin_amdgcn_sched_barrier(0)
constexpr int AUX_NT = 2, AUX_SC1 = 16;
constexpr unsigned OOB = 0x80000000u;

#define RATE 7.85
#define HEAD 10
#define WF 6
#define WL 4
#define W1 3
#define W2 13
#define PBS 3
#define QS 2
#define DLY 25
#define PG 5
#define PGW 10
#define CUP 10
#define CDN 10
#define SY1 19
#define SY2 32
#define QPOLL 520
#define QPH 27
#define ALIGN 1
#define ENDO 140
#define ENDC 150
#define TSLEEP 24
#define L0T 80

constexpr int DH = 128, DM = 512, GG = 4;
constexpr float CSC = 0.088388347648318440f * 1.4426950408889634f;
constexpr float NEG_BIG = -3.4028234663852886e38f;

__device__ __forceinline__ rsrc_t mk(const void* p, unsigned bytes) { return __builtin_amdgcn_make_buffer_rsrc((void*)p, 0, bytes, 0x00020000); }
template <int AUX> __device__ __forceinline__ u32x4 ld128(rsrc_t r, unsigned voff, unsigned soff) { return __builtin_amdgcn_raw_buffer_load_b128(r, voff, soff, AUX); }
__device__ __forceinline__ void st128(u32x4 v, rsrc_t r, unsigned off) { __builtin_amdgcn_raw_buffer_store_b128(v, r, off, 0, AUX_SC1); }
__device__ __forceinline__ f32x4 mfma32(u32x4 a, u32x4 b, f32x4 c) {
  return __builtin_amdgcn_mfma_f32_16x16x32_bf16(__builtin_bit_cast(bf16x8, a), __builtin_bit_cast(bf16x8, b), c, 0, 0, 0);
}
__device__ __forceinline__ f32x4 mfma16(unsigned a0, unsigned a1, unsigned b0, unsigned b1, f32x4 c) {
  u32x2 a = {a0, a1}, b = {b0, b1};
  return __builtin_amdgcn_mfma_f32_16x16x16bf16_1k(__builtin_bit_cast(s16x4, a), __builtin_bit_cast(s16x4, b), c, 0, 0, 0);
}
__device__ __forceinline__ unsigned cvtpk(float lo, float hi) {
  f32x2 v = {lo, hi};
  return __builtin_bit_cast(unsigned, __builtin_convertvector(v, bf16x2));
}
template <int CTRL> __device__ __forceinline__ float dpp(float x) {
  return __builtin_bit_cast(float, __builtin_amdgcn_mov_dpp(__builtin_bit_cast(int, x), CTRL, 0xf, 0xf, true));
}
__device__ __forceinline__ float xrow_max(float x) {
  auto s = __builtin_amdgcn_permlane16_swap(__float_as_uint(x), __float_as_uint(x), false, false);
  x = fmaxf(__uint_as_float(s[0]), __uint_as_float(s[1]));
  auto t = __builtin_amdgcn_permlane32_swap(__float_as_uint(x), __float_as_uint(x), false, false);
  return fmaxf(__uint_as_float(t[0]), __uint_as_float(t[1]));
}
__device__ __forceinline__ float xrow_sum(float x) {
  auto s = __builtin_amdgcn_permlane16_swap(__float_as_uint(x), __float_as_uint(x), false, false);
  x = __uint_as_float(s[0]) + __uint_as_float(s[1]);
  auto t = __builtin_amdgcn_permlane32_swap(__float_as_uint(x), __float_as_uint(x), false, false);
  return __uint_as_float(t[0]) + __uint_as_float(t[1]);
}
__device__ __forceinline__ u32x4 col_frag(const u32x4* W, int d) {
  u32x4 T;
#pragma unroll
  for (int t = 0; t < 4; ++t) T[t] = __builtin_amdgcn_perm(W[2 * t + 1][d >> 1], W[2 * t][d >> 1], (d & 1) ? 0x07060302u : 0x05040100u);
  return T;
}
#define WAIT_LGKM() asm volatile("s_waitcnt lgkmcnt(0)" ::: "memory")
#define WAIT_VM0() asm volatile("s_waitcnt vmcnt(0)" ::: "memory")
template <int N> __device__ __forceinline__ void waitvm() { asm volatile("s_waitcnt vmcnt(%0)" :: "n"(N)); }
__device__ __forceinline__ unsigned now32() { return (unsigned)__builtin_amdgcn_s_memrealtime(); }
__device__ __forceinline__ unsigned pace_until(unsigned target) {
  unsigned late;
  asm volatile("1:\n\ts_memrealtime vcc\n\ts_waitcnt lgkmcnt(0)\n\ts_sub_u32 %0, vcc_lo, %1\n\ts_cmp_lt_i32 %0, 0\n\ts_cbranch_scc0 2f\n\ts_sleep 1\n\ts_branch 1b\n2:"
               : "=&s"(late) : "s"(target) : "vcc", "scc");
  return late;
}

constexpr unsigned TAU = (unsigned)(1.048576 / RATE * 100 * 256 + 0.5);
constexpr unsigned TAUW = TAU + (unsigned)(TAU * WF / (WL * 16)), TAUP = TAU + TAU * PBS / 48, TAUQ = TAU + TAU * QS / 32;
constexpr int PB0 = 9;
constexpr bool in_win(int n) { return (n >= W1 && n < W1 + WL) || (n >= W2 && n < W2 + WL); }
constexpr unsigned tau_of(int n) { return (n == 1 || n == 2) ? TAUQ : in_win(n) ? TAUW : (n >= PB0 && n < PB0 + 3) ? TAUP : TAU; }
constexpr unsigned start_of(int n) { unsigned t = HEAD * TAU; for (int k = 0; k < n; ++k) t += tau_of(k) * 16; return t / 256; }
constexpr unsigned long long win_mask() { unsigned long long m = 0; for (int n = 0; n < 40; ++n) if (in_win(n)) m |= 1ull << n; return m; }
constexpr unsigned PHMAX = TAU * GG / 256;
static_assert(W1 >= 3 && W1 + WL <= PB0 && W2 >= PB0 + 3, "schedule");

struct Pacer {
  unsigned t0, acc, step, gain, ph, tg, seq;
  int adj, corr, left;
  LAS unsigned* org;
  LAS int* tgt; int prev, wv;
  LAS u32x4* cmd;
  __device__ __forceinline__ unsigned gate() {
    const unsigned t = t0 + ((acc + adj) >> 8);
    const unsigned late = pace_until(t);
    tg = t + late;
    acc += step;
    adj += left > 0 ? corr : 0;
    left -= 1;
    return late;
  }
  __device__ __forceinline__ void gate_first(int n) {
    const bool win = (win_mask() >> n) & 1;
    step = (win ? TAUW : (n == 1 || n == 2) ? TAUQ : (n >= PB0 && n < PB0 + 3) ? TAUP : TAU) * GG;
    gain = (win ? PGW : PG) * (TAU * GG / 256);
    const u32x4 cv = *cmd;
    if (ALIGN) {
      const u32x4 tv = *(LAS u32x4*)(tgt + ((n & 1) ^ 1) * 4);
      adj += (__builtin_amdgcn_readfirstlane((int)(tv[0] + tv[1] + tv[2] + tv[3])) >> 2) - prev;
      prev = adj;
      tgt[(n & 1) * 4 + wv] = adj;
    }
    const unsigned cs = __builtin_amdgcn_readfirstlane(cv[0]), co = __builtin_amdgcn_readfirstlane(cv[1]) + ph, lg = __builtin_amdgcn_readfirstlane(cv[3]) & 7u;
    const int want = (((int)(co - t0) << 8) - adj) >> lg;
    const int hi = TAU * GG * CUP / 256, lo = -(int)(TAU * GG * CDN / 256);
    corr = cs != seq ? (want > hi ? hi : want < lo ? lo : want) : corr;
    left = cs != seq ? 1 << lg : left;
    seq = cs;
    const unsigned o = t0 - ph + (adj >> 8);
    const unsigned late = gate();
    *org = o + (late > 14u ? late - 14u : 0u);
  }
  __device__ __forceinline__ void probe() {
    pace_until(tg + DLY);
    const unsigned ib = __builtin_amdgcn_s_getreg(7 | (31 << 11));
    const unsigned vm = (ib & 15u) | ((ib >> 26) & 0x30u);
    const unsigned e = vm > GG ? vm - GG : 0u;
    adj += (e > 4u ? 4u : e) * gain;
  }
};

constexpr unsigned WS_EPOCH = 0;
constexpr unsigned WS_Q = 4096;
constexpr unsigned WS_PB = WS_Q + 8 * 256 * 128;
constexpr unsigned WS_OUTP = WS_PB + 256 * 2048;
constexpr unsigned WS_SY = WS_OUTP + 256 * 4096;
constexpr unsigned WS_END = WS_SY + 2 * 256 * 128;
#define STAMP(k) do { if ((k) == DIAG) dstamp = now32(); } while (0)
#define STAMP_DECL const unsigned tstart = now32(); unsigned dstamp = 0
#define STAMP_FLUSH() do { if ((wave >= 4) == DIAG_ROLE && dstamp) { const unsigned tg = tstart + 15000 + DIAG_SGN * 10 * (int)(dstamp - tstart - DIAG_SUB); while ((int)(now32() - tg) < 0) __builtin_amdgcn_s_sleep(4); } } while (0)
constexpr int L_QF = 0;
constexpr int L_PB = L_QF + 2048;
constexpr int L_MLB = L_PB + 8192;
constexpr int L_PA = L_MLB + 128;
constexpr int L_MLA = L_PA + 1024;
constexpr int L_GB = L_MLA + 32;
constexpr int L_GML = L_GB + 1024;
constexpr int L_TAG = L_GML + 32;
constexpr int L_SYNC = L_TAG + 16;
constexpr int L_CMD = L_SYNC + 32, L_ORG = L_CMD + 16;
constexpr int L_TGT = L_ORG + 16;
constexpr int L_SC = L_TGT + 32;
constexpr int L_QP = L_SC + 16384;
constexpr int L_USED = L_QP + 16384;
constexpr int L_END = 96 * 1024;
static_assert(L_USED <= L_END, "LDS map");
enum { S_QP_CNT = 0, S_QB_READY = 1, S_QA_READY = 2, S_BP_CNT = 3, S_ML_CNT = 4, S_PB_SEEN = 5 };

__device__ __forceinline__ void lds_wait_ge(LAS unsigned* w, unsigned target) {
#pragma unroll 1
  for (unsigned spins = 0; spins < (1u << 22); ++spins) {
    const unsigned v = __builtin_amdgcn_readfirstlane(__hip_atomic_load(w, __ATOMIC_RELAXED, __HIP_MEMORY_SCOPE_WORKGROUP));
    if (v >= target) break;
    __builtin_amdgcn_s_sleep(1);
  }
  asm volatile("" ::: "memory");
}
__device__ __forceinline__ void lds_signal(LAS unsigned* w, int lane) {
  WAIT_LGKM();
  if (lane == 0) __hip_atomic_fetch_add(w, 1u, __ATOMIC_RELAXED, __HIP_MEMORY_SCOPE_WORKGROUP);
}

constexpr int U = 16;
constexpr unsigned STEP = 65536;
__device__ __forceinline__ float quad_diag(const f32x4 d) { return ((dpp<0x00>(d[0]) + dpp<0x55>(d[1])) + dpp<0xAA>(d[2])) + dpp<0xFF>(d[3]); }
__device__ __forceinline__ unsigned ld_off(const unsigned (&lo)[4], int i) { return lo[i >> 2] + (i & 1) * 4096 + ((i >> 1) & 1) * 32768; }
struct Next { rsrc_t r; unsigned v, so; int n; };
__device__ __forceinline__ void reload(u32x4& R, int i, const Next& nx, const unsigned (&lo)[4], Pacer& pc) {
  if (i == 0) pc.gate_first(nx.n); else if (i % GG == 0) pc.gate();
  R = ld128<AUX_NT>(nx.r, nx.v, nx.so + ld_off(lo, i));
}

__device__ __forceinline__ void k_step(u32x4 (&R)[U], const u32x4 Qf, float& mx, LAS unsigned char* sc, bool writer,
                                       const Next& nx, const unsigned (&lo)[4], Pacer& pc) {
  float s[U];
  f32x4 d[U];
  const f32x4 z = {0, 0, 0, 0};
#pragma unroll
  for (int i = 0; i < U; ++i) {
    d[i] = mfma32(R[i], Qf, z);
    reload(R[i], i, nx, lo, pc);
    if (i > 0) s[i - 1] = quad_diag(d[i - 1]);
    SB();
    if (i % GG == GG - 1 && i < U - 1) { pc.probe(); SB(); }
  }
  s[U - 1] = quad_diag(d[U - 1]);
#pragma unroll
  for (int i = 0; i < U; ++i) mx = fmaxf(mx, s[i]);
  if (writer) {
#pragma unroll
    for (int t = 0; t < 4; ++t) *(LAS f32x4*)(sc + t * 16) = f32x4{s[4 * t], s[4 * t + 1], s[4 * t + 2], s[4 * t + 3]};
  }
  SB();
}
__device__ __forceinline__ void k_step_first(u32x4 (&R)[U], const u32x4 (&X0)[U], const u32x4 (&X1)[U], const u32x4 Qf, float& mx, LAS unsigned char* sc, bool writer,
                                             const Next& nx, const unsigned (&lo)[4], Pacer& pc) {
  const f32x4 z = {0, 0, 0, 0};
#pragma unroll
  for (int t = 0; t < 4; ++t) {
    f32x4 d[4];
#pragma unroll
    for (int u = 0; u < 4; ++u) {
      d[u] = mfma32(R[4 * t + u], Qf, z);
      reload(R[4 * t + u], 4 * t + u, nx, lo, pc);
      SB();
    }
    f32x4 s2, s0, s1;
#pragma unroll
    for (int u = 0; u < 4; ++u) { s2[u] = quad_diag(d[u]); s0[u] = quad_diag(mfma32(X0[4 * t + u], Qf, z)); s1[u] = quad_diag(mfma32(X1[4 * t + u], Qf, z)); }
#pragma unroll
    for (int u = 0; u < 4; ++u) mx = fmaxf(mx, fmaxf(s2[u], fmaxf(s0[u], s1[u])));
    if (writer) { *(LAS f32x4*)(sc + t * 16) = s0; *(LAS f32x4*)(sc + 1024 + t * 16) = s1; *(LAS f32x4*)(sc + 2048 + t * 16) = s2; }
    asm volatile("" : "+v"(mx));
    SB();
  }
}
struct Acc { float l; f32x4 lo, hi; };
__device__ __forceinline__ void p_step(unsigned (&a0)[U], unsigned (&a1)[U], float mc, float& l, LAS unsigned char* sc, unsigned mask0, unsigned mask1) {
  f32x4 s4[4];
#pragma unroll
  for (int t = 0; t < 4; ++t) s4[t] = *(LAS f32x4*)(sc + t * 16);
#pragma unroll
  for (int i = 0; i < U; ++i) {
    const float p = __builtin_amdgcn_exp2f(fmaf(s4[i >> 2][i & 3], CSC, -mc));
    l += p;
    const unsigned t = cvtpk(p, p);
    a0[i] = t & mask0; a1[i] = t & mask1;
  }
#pragma unroll
  for (int i = 0; i < U; ++i) asm volatile("" : "+v"(a0[i]), "+v"(a1[i]));
  asm volatile("" : "+v"(l));
  SB();
}
template <bool RELOAD>
__device__ __forceinline__ void v_step(u32x4 (&R)[U], const unsigned (&a0)[U], const unsigned (&a1)[U], Acc& a,
                                       const Next& nx, const unsigned (&lo)[4], Pacer& pc) {
#pragma unroll
  for (int i = 0; i < U; ++i) {
    a.lo = mfma16(a0[i], a1[i], R[i][0], R[i][1], a.lo);
    a.hi = mfma16(a0[i], a1[i], R[i][2], R[i][3], a.hi);
    if (RELOAD) reload(R[i], i, nx, lo, pc);
    SB();
    if (RELOAD && i % GG == GG - 1 && i < U - 1) { pc.probe(); SB(); }
  }
}
__device__ __forceinline__ void issue_step(u32x4 (&R)[U], const Next& nx, const unsigned (&lo)[4], Pacer& pc) {
#pragma unroll
  for (int i = 0; i < U; ++i) {
    if (i % GG == 0) waitvm<12>();
    reload(R[i], i, nx, lo, pc);
    SB();
    if (i % GG == GG - 1 && i < U - 1) { pc.probe(); SB(); }
  }
}

__device__ __forceinline__ unsigned my_origin(LAS unsigned char* lds) {
  const u32x4 o = *(LAS u32x4*)(lds + L_ORG);
  return o[0] + ((int)(o[1] - o[0]) + (int)(o[2] - o[0]) + (int)(o[3] - o[0])) / 4;
}
__device__ __forceinline__ void agree(LAS unsigned char* lds, unsigned mine, unsigned theirs, bool ok, unsigned seq, unsigned lg, int lane) {
  float d = (ok && lane < 32) ? (float)(int)(theirs - mine) : 0.f;
  d += dpp<0xB1>(d); d += dpp<0x4E>(d); d += dpp<0x141>(d); d += dpp<0x128>(d);
  d = xrow_sum(d);
  const int n = __builtin_popcount((unsigned)__builtin_amdgcn_ballot_w64(ok));
  if (lane == 0) *(LAS u32x4*)(lds + L_CMD) = u32x4{seq, mine + (int)(d * __builtin_amdgcn_rcpf((float)(n > 0 ? n : 1))), 0u, lg};
}
__device__ __forceinline__ void agree_round(LAS unsigned char* lds, rsrc_t rws, unsigned when, int round, unsigned seq, unsigned lg, unsigned tagA, unsigned tagB, int j, int lane) {
  pace_until(when);
  const unsigned mine = my_origin(lds);
  if (lane < 8) st128(u32x4{mine, tagA, mine, tagB}, rws, WS_SY + (round * 256 + j) * 128 + lane * 16);
  pace_until(when + 250);
  const u32x4 f = ld128<AUX_SC1>(rws, (lane & 31) * 128, WS_SY + (round * 256 + (j & ~31)) * 128);
  agree(lds, mine, f[0], f[1] == tagA && f[3] == tagB, seq, lg, lane);
}

template <int WS>
__device__ __forceinline__ void wo_load(u32x4 (&T)[32], const uint16_t* wo, int h, int hw, int g, int c, unsigned th0) {
  constexpr int P = (start_of(WS + WL) - start_of(WS)) / 39;
  const unsigned ts = th0 + start_of(WS) + (h >> 3) * 2 * P + (h & 7) * (P / 8);
  rsrc_t rw = mk(wo + (size_t)h * DH * DM, DH * DM * 2);
  const unsigned vo = (unsigned)(8 * g) * 1024u + hw * 256 + c * 16;
  u32x4 W[32];
#pragma unroll
  for (int i = 0; i < 32; ++i) {
    pace_until(ts + i * P);
    W[i] = ld128<AUX_NT>(rw, vo + (i & 3) * 1024, (32 * (i >> 3) + (i & 4)) * 1024); SB();
  }
  __builtin_amdgcn_s_setprio(0);
#pragma unroll
  for (int s = 0; s < 4; ++s) {
#pragma unroll
    for (int d = 0; d < 8; ++d) T[8 * s + d] = col_frag(W + 8 * s, d);
#pragma unroll
    for (int d = 0; d < 8; ++d) asm volatile("" : "+v"(T[8 * s + d]));
    SB();
  }
  __builtin_amdgcn_s_setprio(3);
}
__device__ __forceinline__ f32x4 merge_weights(const f32x4 m4, const f32x4 l4) {
  const float M = fmaxf(fmaxf(m4[0], m4[1]), fmaxf(m4[2], m4[3]));
  f32x4 w4;
#pragma unroll
  for (int t = 0; t < 4; ++t) w4[t] = __builtin_amdgcn_exp2f(m4[t] - M);
  const float L = (w4[0] * l4[0] + w4[1] * l4[1]) + (w4[2] * l4[2] + w4[3] * l4[3]);
  return w4 * (1.0f / L);
}
__device__ __forceinline__ u32x4 project4(const u32x4 (&A)[4], const u32x4 (&T)[32], const f32x4 w4) {
  f32x4 D[8];
#pragma unroll
  for (int d = 0; d < 8; ++d) D[d] = f32x4{0, 0, 0, 0};
#pragma unroll
  for (int s = 0; s < 4; ++s)
#pragma unroll
    for (int d = 0; d < 8; ++d) D[d] = mfma32(A[s], T[s * 8 + d], D[d]);
  float o[8];
#pragma unroll
  for (int d = 0; d < 8; ++d) o[d] = (D[d][0] * w4[0] + D[d][1] * w4[1]) + (D[d][2] * w4[2] + D[d][3] * w4[3]);
  return u32x4{cvtpk(o[0], o[1]), cvtpk(o[2], o[3]), cvtpk(o[4], o[5]), cvtpk(o[6], o[7])};
}
__device__ __forceinline__ u32x2 tags_of(u32x2 ep) {
  const unsigned long long t = (((unsigned long long)ep[1] << 32) | ep[0]) + 1ull;
  return u32x2{(unsigned)t, (unsigned)(t >> 32) ^ ((unsigned)t * 0x9E3779B1u)};
}

__global__ void __launch_bounds__(512, 2) k_fused(const uint16_t* xA, const uint16_t* KA, const uint16_t* VA, const uint16_t* wqA, const uint16_t* woA,
                                                  const uint16_t* xB, const uint16_t* KB, const uint16_t* VB, const uint16_t* wqB, const uint16_t* woB,
                                                  uint16_t* out, unsigned char* ws) {
  __shared__ __attribute__((aligned(16))) unsigned char smem[L_END];
  LAS unsigned char* lds = (LAS unsigned char*)smem;
  LAS unsigned* sync = (LAS unsigned*)(lds + L_SYNC);
  const int lane = threadIdx.x & 63, wave = __builtin_amdgcn_readfirstlane(threadIdx.x >> 6);
  const int j = blockIdx.x, b = j >> 5, h = j & 31;
  const int g = lane >> 4, c = lane & 15, jj = c & 3;
  const unsigned ph = (j >> 3) * (TAU * GG / 32) / 256;
  STAMP_DECL;
  STAMP(0);
  if (wave < 4) {
    constexpr unsigned AB = 1u << 20, BB = 1u << 18;
    __builtin_amdgcn_s_setprio(2);
    const unsigned kv = ((lane >> 2) & 3) * 256 + (4 * g + (lane & 3)) * 16, vv = lane * 16;
    const rsrc_t rkA = mk((const char*)KA + (size_t)j * AB, AB), rvA = mk((const char*)VA + (size_t)j * AB, AB);
    const rsrc_t rkB = mk((const char*)KB + (size_t)j * BB, BB), rvB = mk((const char*)VB + (size_t)j * BB, BB);
    STAMP(11);
    Pacer pc; pc.t0 = now32() + ph; pc.acc = HEAD * TAU; pc.step = 0; pc.gain = 0; pc.ph = ph; pc.tg = 0; pc.seq = 0;
    pc.adj = 0; pc.corr = 0; pc.left = 0; pc.org = (LAS unsigned*)(lds + L_ORG) + wave; pc.cmd = (LAS u32x4*)(lds + L_CMD);
    {
      const unsigned x = __builtin_amdgcn_raw_buffer_load_b32(wave == 0 ? rkB : wave == 1 ? rvB : wave == 2 ? rkA : rvA, lane == 0 ? 0u : OOB, 0, AUX_NT);
      __builtin_amdgcn_s_barrier();
      asm volatile("" :: "v"(x) : "memory");
      if (ALIGN) pc.t0 = __builtin_amdgcn_readfirstlane(*pc.org) + ph;
      const int over = (int)(now32() - (pc.t0 - ph)) - L0T;
      pc.adj = over > 0 ? over << 8 : 0;
      pc.tgt = (LAS int*)(lds + L_TGT); pc.prev = 0; pc.wv = wave;
      STAMP(13);
    }
    unsigned lo[4];
#pragma unroll
    for (int i = 0; i < 4; ++i) lo[i] = wave * 1024 + ((i ^ ((__builtin_popcount(j & 0x13) & 1) | ((__builtin_popcount(j & 0x2c) & 1) << 1))) << 13);
    u32x4 R[U], X0[U], X1[U];
    issue_step(X0, Next{rkB, kv, 0, 0}, lo, pc);
    issue_step(X1, Next{rkB, kv, STEP, 1}, lo, pc);
    issue_step(R, Next{rkB, kv, 2 * STEP, 2}, lo, pc);
    STAMP(5);
    lds_wait_ge(sync + S_QB_READY, 1);
    u32x4 Qf = *(LAS u32x4*)(lds + L_QF + lane * 16);
    STAMP(1);
    LAS unsigned char* scB = lds + L_SC + wave * 4096 + (g * 4 + (c >> 2)) * 64;
    LAS unsigned char* scA = lds + L_SC + wave * 4096 + g * 64;
    const unsigned mask0 = jj == 0 ? 0x0000ffffu : jj == 1 ? 0xffff0000u : 0u, mask1 = jj == 2 ? 0x0000ffffu : jj == 3 ? 0xffff0000u : 0u;
    float mx = NEG_BIG, mc = 0.f;
    k_step_first(R, X0, X1, Qf, mx, scB, jj == 0, Next{rkB, kv, 3 * STEP, 3}, lo, pc);
    STAMP(16); STAMP(17); STAMP(18);
    Acc a;
    unsigned a0[U], a1[U];
    LAS unsigned char* sc = scB + 3 * 1024;
    int p = 3;
#pragma unroll 1
    for (int br = 0; br < 2; ++br) {
      const int kend = br ? 24 : 4, vend = br ? 39 : 8, sstep = br ? 256 : 1024;
      const bool writer = br ? c == 0 : jj == 0;
      const rsrc_t rv = br ? rvA : rvB;
#pragma unroll 1
      for (; p < kend; ++p) {
        const int n = p + 1;
        const bool last = n == kend;
        k_step(R, Qf, mx, sc, writer, Next{last ? rv : rkA, last ? vv : kv, (unsigned)__builtin_amdgcn_readfirstlane(last ? 0 : (n - 8) * STEP), n}, lo, pc);
        STAMP(16 + p);
        sc += sstep;
      }
      mc = xrow_max(mx) * CSC;
      a.l = 0.f; a.lo = f32x4{0, 0, 0, 0}; a.hi = a.lo;
      sc = br ? scA : scB;
#pragma unroll 1
      for (; p < vend; ++p) {
        const int n = p + 1;
        const bool last = n == 8;
        p_step(a0, a1, mc, a.l, sc, mask0, mask1);
        v_step<true>(R, a0, a1, a, Next{last ? rkA : rv, last ? kv : vv, (unsigned)__builtin_amdgcn_readfirstlane(last ? 0 : (n - kend) * STEP), n}, lo, pc);
        STAMP(16 + p);
        sc += sstep;
      }
      if (br == 0) {
        const float lt = xrow_sum(a.l);
        *(LAS f32x4*)(lds + L_PB + wave * 2048 + lane * 32) = a.lo; *(LAS f32x4*)(lds + L_PB + wave * 2048 + lane * 32 + 16) = a.hi;
        if (g == 0 && jj == 0) { *(LAS float*)(lds + L_MLB + (c >> 2) * 32 + wave * 4) = mc; *(LAS float*)(lds + L_MLB + (c >> 2) * 32 + 16 + wave * 4) = lt; }
        lds_signal(sync + S_BP_CNT, lane);
        STAMP(3);
        lds_wait_ge(sync + S_QA_READY, 1);
        Qf = *(LAS u32x4*)(lds + L_QF + 1024 + lane * 16);
        mx = NEG_BIG; sc = scA;
      }
    }
    p_step(a0, a1, mc, a.l, sc, mask0, mask1);
    {
      const float lt = xrow_sum(a.l);
      if (lane == 0) { *(LAS float*)(lds + L_MLA + wave * 4) = mc; *(LAS float*)(lds + L_MLA + 16 + wave * 4) = lt; }
      lds_signal(sync + S_ML_CNT, lane);
    }
    const rsrc_t rws = mk(ws, WS_END), rout = mk(out, 8 * 1024 * 2);
    unsigned in_off = WS_OUTP + (b * 32 + 8 * g) * 4096 + h * 128 + c * 8;
    unsigned outp = g == 0 ? (b * 1024 + (c >> 3) * 512 + h * 16 + (c & 7) * 2) * 2 : OOB;
    unsigned mytag = *(LAS unsigned*)(lds + L_TAG + (c & 1) * 4);
    u32x2 next_ep = {*(LAS unsigned*)(lds + L_TAG), *(LAS unsigned*)(lds + L_TAG + 8)};
    unsigned ep_off = (h == 0 && lane == 0) ? WS_EPOCH + b * 256 : OOB;
    const unsigned phb = ((j >> 3) | 3) * (TAU * GG / 32) / 256;
    unsigned tp = __builtin_amdgcn_readfirstlane((*pc.cmd)[1]) + (start_of(40) - PHMAX + ENDC) + phb;
    const unsigned tq = pc.tg - ph + phb + ENDO;
    tp = (tp - tq) < 200u ? tp : tq;
    asm volatile("" : "+v"(in_off), "+v"(outp), "+v"(mytag), "+v"(next_ep), "+v"(ep_off));
    SB();
    v_step<false>(R, a0, a1, a, Next{rvA, vv, 0, 0}, lo, pc);
    if (g == 0) *(LAS u32x4*)(lds + L_PA + wave * 256 + c * 16) = u32x4{cvtpk(a.lo[0], a.lo[1]), cvtpk(a.lo[2], a.lo[3]), cvtpk(a.hi[0], a.hi[1]), cvtpk(a.hi[2], a.hi[3])};
    WAIT_LGKM();
    __builtin_amdgcn_s_barrier();
    STAMP(55);
    STAMP(2);
    if (wave == 0) {
      __builtin_amdgcn_s_sleep(TSLEEP);
      pace_until(tp);
      unsigned v[8];
#pragma unroll 1
      for (unsigned spins = 0; spins < (1u << 18); ++spins) {
        u32x2 t[8];
        asm volatile("" : "+v"(in_off) :: "memory");
#pragma unroll
        for (int i = 0; i < 8; ++i) t[i] = __builtin_amdgcn_raw_buffer_load_b64(rws, in_off, i * 4096, AUX_SC1);
        unsigned bad = 0;
#pragma unroll
        for (int i = 0; i < 8; ++i) { v[i] = t[i][0]; bad |= t[i][1] ^ mytag; }
        if (__builtin_amdgcn_ballot_w64(bad != 0) == 0) break;
      }
      float e[8], o[8];
#pragma unroll
      for (int i = 0; i < 8; ++i) { e[i] = __uint_as_float(v[i] << 16); o[i] = __uint_as_float(v[i] & 0xffff0000u); }
      float s0 = ((e[0] + e[1]) + (e[2] + e[3])) + ((e[4] + e[5]) + (e[6] + e[7])), s1 = ((o[0] + o[1]) + (o[2] + o[3])) + ((o[4] + o[5]) + (o[6] + o[7]));
      s0 = xrow_sum(s0); s1 = xrow_sum(s1);
      __builtin_amdgcn_raw_buffer_store_b32(cvtpk(s0, s1), rout, outp, 0, 0);
      __builtin_amdgcn_raw_buffer_store_b64(next_ep, rws, ep_off, 0, AUX_SC1);
      STAMP(12);
    }
    STAMP(9);
  } else {
    const int hw = wave - 4;
    __builtin_amdgcn_s_setprio(3);
    const rsrc_t rws = mk(ws, WS_END);
    asm volatile("" :: "s"(ws));
    SB();
    const unsigned th0 = now32();
    const int pq = (j & 7) * 32 + (j >> 3);
    u32x2 epv;
    {
      const rsrc_t rw = mk(((pq & 128) ? wqB : wqA) + (pq & 126) * 32, (unsigned)(DM * 4096 * 2 - (pq & 126) * 64));
      const rsrc_t rx = mk((pq & 128) ? xB : xA, 8 * DM * 2);
      const unsigned row0 = 128 * hw + 16 * g + 8 * (c >> 3);
      const unsigned vo = row0 * 8192u + (c & 7) * 16;
      const unsigned tw = th0 + ((j >> 4) * QPH) / 16;
      u32x4 W[16];
      pace_until(tw);
#pragma unroll
      for (int k = 0; k < 4; ++k) { const int i = (k >> 1) + (k & 1) * 4; W[i] = ld128<AUX_NT>(rw, vo, i * 8192); SB(); }
      const unsigned xo = (c & 7) * (DM * 2) + row0 * 2;
      const u32x4 A0 = ld128<AUX_NT>(rx, xo, 0), A1 = ld128<AUX_NT>(rx, xo, 128);
      epv = __builtin_amdgcn_raw_buffer_load_b64(rws, (lane >> 3) * 256, WS_EPOCH, AUX_SC1);
      if (hw == 0 && lane < 24) sync[lane] = (lane >> 2) == 3 ? th0 : 0u;
      WAIT_LGKM();
      __builtin_amdgcn_s_barrier();
      SB();
#pragma unroll
      for (int k = 4; k < 16; ++k) {
        if (k % 4 == 0) pace_until(tw + (k / 4) * QPH);
        const int i = (k & 8) + ((k & 7) >> 1) + (k & 1) * 4;
        W[i] = ld128<AUX_NT>(rw, vo, ((i >> 3) * 64 + (i & 7)) * 8192); SB();
      }
      const f32x4 z = {0, 0, 0, 0};
      f32x4 D[8];
#pragma unroll
      for (int d = 0; d < 8; ++d) D[d] = mfma32(A0, col_frag(W, d), z);
#pragma unroll
      for (int d = 0; d < 8; ++d) D[d] = mfma32(A1, col_frag(W + 8, d), D[d]);
      STAMP(25);
      if ((g >> 1) == (c >> 3) && ((c >> 2) & 1) == (pq & 1)) {
#pragma unroll
        for (int t = 0; t < 4; ++t) {
          LAS unsigned char* qp = lds + L_QP + (hw * 2 + (c >> 3)) * 1024 + (4 * (g & 1) + t) * 128 + jj * 32;
          *(LAS f32x4*)qp = f32x4{D[0][t], D[1][t], D[2][t], D[3][t]}; *(LAS f32x4*)(qp + 16) = f32x4{D[4][t], D[5][t], D[6][t], D[7][t]};
        }
      }
    }
    lds_signal(sync + S_QP_CNT, lane);
    const u32x2 tgv = tags_of(epv);
    const unsigned tagA = __builtin_amdgcn_readlane(tgv[0], 8 * b), tagB = __builtin_amdgcn_readlane(tgv[1], 8 * b);
    if (hw == 0) {
      lds_wait_ge(sync + S_QP_CNT, 4);
      f32x4 s[8];
#pragma unroll
      for (int k = 0; k < 8; ++k) s[k] = *(LAS f32x4*)(lds + L_QP + k * 1024 + lane * 16);
      const f32x4 q = ((s[0] + s[1]) + (s[2] + s[3])) + ((s[4] + s[5]) + (s[6] + s[7]));
      st128(u32x4{cvtpk(q[0], q[1]), tgv[0], cvtpk(q[2], q[3]), tgv[1]}, rws, WS_Q + ((lane >> 3) * 256 + pq) * 128 + (lane & 7) * 16);
      if (lane == 8 * b) { *(LAS unsigned*)(lds + L_TAG) = tagA; *(LAS unsigned*)(lds + L_TAG + 4) = tagB; *(LAS unsigned*)(lds + L_TAG + 8) = epv[1] + (tagA == 0u ? 1u : 0u); }
      STAMP(10);
    }
    if (hw == 1 || hw == 2) {
      unsigned qo = WS_Q + (b * 256 + (hw == 1 ? 128 + 4 * ((h & ~3) + (c >> 2)) : 4 * h) + g) * 128 + jj * 32;
      u32x4 f0, f1;
      pace_until(th0 + (hw == 1 ? QPOLL : QPOLL + 200));
#pragma unroll 1
      for (unsigned spins = 0; spins < (1u << 17); ++spins) {
        asm volatile("" : "+v"(qo) :: "memory");
        f0 = ld128<AUX_SC1>(rws, qo, 0); f1 = ld128<AUX_SC1>(rws, qo + 16, 0);
        if (__builtin_amdgcn_ballot_w64(f0[1] != tagA || f0[3] != tagB || f1[1] != tagA || f1[3] != tagB) == 0) break;
        __builtin_amdgcn_s_sleep(16);
      }
      *(LAS u32x4*)(lds + L_QF + (hw - 1) * 1024 + lane * 16) = u32x4{f0[0], f0[2], f1[0], f1[2]};
      lds_signal(sync + (hw == 1 ? S_QB_READY : S_QA_READY), lane);
      STAMP(1);
    }
    if (hw == 3) {
      const char* pt = lane == 0 ? (const char*)out : lane == 1 ? (const char*)woA + (size_t)h * 131072 : (const char*)woB + (size_t)h * 131072;
      const unsigned x = __builtin_nontemporal_load((const __attribute__((address_space(1))) unsigned*)pt);
      asm volatile("" :: "v"(x));
    }
    u32x4 T[32];
    wo_load<W1>(T, woB, h, hw, g, c, th0);
    STAMP(2);
    if (hw == 0) {
      lds_wait_ge(sync + S_BP_CNT, 4);
      STAMP(4);
      const f32x4 m4 = *(LAS f32x4*)(lds + L_MLB + g * 32), l4 = *(LAS f32x4*)(lds + L_MLB + g * 32 + 16);
      const float M = fmaxf(fmaxf(m4[0], m4[1]), fmaxf(m4[2], m4[3]));
      f32x4 a0 = {0, 0, 0, 0}, a1 = a0; float L = 0.f;
#pragma unroll
      for (int w = 0; w < 4; ++w) {
        const float e = __builtin_amdgcn_exp2f(m4[w] - M); L += e * l4[w];
        a0 += e * *(LAS f32x4*)(lds + L_PB + w * 2048 + lane * 32); a1 += e * *(LAS f32x4*)(lds + L_PB + w * 2048 + lane * 32 + 16);
      }
      const u32x4 pk = {cvtpk(a0[0], a0[1]), cvtpk(a0[2], a0[3]), cvtpk(a1[0], a1[1]), cvtpk(a1[2], a1[3])};
      const bool own = g == (h & 3);
      if (!own) st128(pk, rws, WS_PB + j * 2048 + lane * 16);
      const unsigned mine = my_origin(lds);
      WAIT_VM0();
      if (c < 2) st128(c == 0 ? u32x4{__float_as_uint(M), tagA, __float_as_uint(L), tagB} : u32x4{mine, tagA, mine, tagB}, rws, WS_PB + j * 2048 + 1024 + c * 64 + g * 16);
      unsigned mo = (lane & 31) * 2048 + 1024 + ((lane & 32) ? (h & 3) * 16 : 64);
      const bool needed = ((lane ^ j) & 28) == 0;
      u32x4 f;
#pragma unroll 1
      for (unsigned spins = 0; spins < (1u << 17); ++spins) {
        __builtin_amdgcn_s_sleep(64);
        asm volatile("" : "+v"(mo) :: "memory");
        f = ld128<AUX_SC1>(rws, mo, WS_PB + (j & ~31) * 2048);
        if (__builtin_amdgcn_ballot_w64(needed && (f[1] != tagA || f[3] != tagB)) == 0) break;
      }
      asm volatile("" ::: "memory");
      const u32x4 v = ld128<AUX_SC1>(rws, own ? OOB : g * 2048 + (h & 3) * 256 + c * 16, WS_PB + (j & ~3) * 2048);
      agree(lds, mine, f[0], f[1] == tagA && f[3] == tagB, 1u, 5u, lane);
      *(LAS u32x4*)(lds + L_GB + lane * 16) = own ? pk : v;
      if (lane >= 32 && needed) { *(LAS unsigned*)(lds + L_GML + (lane & 3) * 4) = f[0]; *(LAS unsigned*)(lds + L_GML + 16 + (lane & 3) * 4) = f[2]; }
      lds_signal(sync + S_PB_SEEN, lane);
    }
    lds_wait_ge(sync + S_PB_SEEN, 1);
    __builtin_amdgcn_s_setprio(0);
    u32x2 keepB;
    {
      u32x4 A[4];
#pragma unroll
      for (int s = 0; s < 4; ++s) A[s] = *(LAS u32x4*)(lds + L_GB + jj * 256 + g * 16 + s * 64);
      const u32x4 o = project4(A, T, merge_weights(*(LAS f32x4*)(lds + L_GML), *(LAS f32x4*)(lds + L_GML + 16)));
      keepB = (g & 2) ? u32x2{o[2], o[3]} : u32x2{o[0], o[1]};
    }
    asm volatile("" : "+v"(keepB));
    __builtin_amdgcn_s_setprio(3);
    STAMP(5);
    SB();
    wo_load<W2>(T, woA, h, hw, g, c, th0);
    STAMP(6);
    if (hw == 1) {
      if (SY1) agree_round(lds, rws, th0 + start_of(SY1), 0, 2u, 5u, tagA, tagB, j, lane);
      if (SY2) agree_round(lds, rws, th0 + start_of(SY2), 1, 3u, 4u, tagA, tagB, j, lane);
    }
    unsigned op_off = WS_OUTP + j * 4096 + (8 * hw + (c >> 1)) * 128 + (g & 1) * 64 + (c & 1) * 32 + (g >> 1) * 16;
    lds_wait_ge(sync + S_ML_CNT, 4);
    f32x4 w4 = merge_weights(*(LAS f32x4*)(lds + L_MLA), *(LAS f32x4*)(lds + L_MLA + 16));
    asm volatile("" : "+v"(w4), "+v"(op_off));
    SB();
    __builtin_amdgcn_s_barrier();
    asm volatile("" ::: "memory");
    STAMP(7);
    {
      u32x4 A[4];
#pragma unroll
      for (int s = 0; s < 4; ++s) A[s] = *(LAS u32x4*)(lds + L_PA + jj * 256 + g * 16 + s * 64);
      SB();
      const u32x4 o = project4(A, T, w4);
      const u32x2 keepA = (g & 2) ? u32x2{o[2], o[3]} : u32x2{o[0], o[1]};
      const u32x2 k = (g & 1) ? keepB : keepA;
      st128(u32x4{k[0], tagA, k[1], tagB}, rws, op_off);
    }
    STAMP(8);
  }
  STAMP_FLUSH();
}

extern "C" void kernel_launch(void* const* d_in, const int* in_sizes, int n_in, void* d_out, int out_size, void* d_ws, size_t ws_size, hipStream_t stream) {
  k_fused<<<256, 512, 0, stream>>>((const uint16_t*)d_in[0], (const uint16_t*)d_in[1], (const uint16_t*)d_in[2], (const uint16_t*)d_in[3], (const uint16_t*)d_in[4],
                                   (const uint16_t*)d_in[5], (const uint16_t*)d_in[6], (const uint16_t*)d_in[7], (const uint16_t*)d_in[8], (const uint16_t*)d_in[9],
                                   (uint16_t*)d_out, (unsigned char*)d_ws);
}
```

```cpp
#define WF 6
#include <hip/hip_runtime.h>
#include <stdint.h>

typedef unsigned u32x4 __attribute__((ext_vector_type(4)));
typedef unsigned u32x2 __attribute__((ext_vector_type(2)));
typedef float f32x4 __attribute__((ext_vector_type(4)));
typedef float f32x2 __attribute__((ext_vector_type(2)));
typedef short s16x4 __attribute__((ext_vector_type(4)));
typedef __bf16 bf16x8 __attribute__((ext_vector_type(8)));
typedef __bf16 bf16x2 __attribute__((ext_vector_type(2)));
typedef __amdgpu_buffer_rsrc_t rsrc_t;
#define LAS __attribute__((address_space(3)))
#define SB() __builtin_amdgcn_sched_barrier(0)
constexpr int AUX_NT = 2, AUX_SC1 = 16;
constexpr unsigned OOB = 0x80000000u;

#define RATE 7.85
#define HEAD 10
#define WL 4
#define W1 3
#define W2 13
#define PBS 3
#define QS 2
#define DLY 25
#define PG 5
#define PGW 10
#define CUP 10
#define CDN 10
#define SY1 19
#define SY2 32
#define QPOLL 520
#define QPH 27
#define ALIGN 1
#define ENDO 140
#define ENDC 150
#define TSLEEP 24
#define L0T 80

constexpr int DH = 128, DM = 512, GG = 4;
constexpr float CSC = 0.088388347648318440f * 1.4426950408889634f;
constexpr float NEG_BIG = -3.4028234663852886e38f;

__device__ __forceinline__ rsrc_t mk(const void* p, unsigned bytes) { return __builtin_amdgcn_make_buffer_rsrc((void*)p, 0, bytes, 0x00020000); }
template <int AUX> __device__ __forceinline__ u32x4 ld128(rsrc_t r, unsigned voff, unsigned soff) { return __builtin_amdgcn_raw_buffer_load_b128(r, voff, soff, AUX); }
__device__ __forceinline__ void st128(u32x4 v, rsrc_t r, unsigned off) { __builtin_amdgcn_raw_buffer_store_b128(v, r, off, 0, AUX_SC1); }
__device__ __forceinline__ f32x4 mfma32(u32x4 a, u32x4 b, f32x4 c) {
  return __builtin_amdgcn_mfma_f32_16x16x32_bf16(__builtin_bit_cast(bf16x8, a), __builtin_bit_cast(bf16x8, b), c, 0, 0, 0);
}
__device__ __forceinline__ f32x4 mfma16(unsigned a0, unsigned a1, unsigned b0, unsigned b1, f32x4 c) {
  u32x2 a = {a0, a1}, b = {b0, b1};
  return __builtin_amdgcn_mfma_f32_16x16x16bf16_1k(__builtin_bit_cast(s16x4, a), __builtin_bit_cast(s16x4, b), c, 0, 0, 0);
}
__device__ __forceinline__ unsigned cvtpk(float lo, float hi) {
  f32x2 v = {lo, hi};
  return __builtin_bit_cast(unsigned, __builtin_convertvector(v, bf16x2));
}
template <int CTRL> __device__ __forceinline__ float dpp(float x) {
  return __builtin_bit_cast(float, __builtin_amdgcn_mov_dpp(__builtin_bit_cast(int, x), CTRL, 0xf, 0xf, true));
}
__device__ __forceinline__ float xrow_max(float x) {
  auto s = __builtin_amdgcn_permlane16_swap(__float_as_uint(x), __float_as_uint(x), false, false);
  x = fmaxf(__uint_as_float(s[0]), __uint_as_float(s[1]));
  auto t = __builtin_amdgcn_permlane32_swap(__float_as_uint(x), __float_as_uint(x), false, false);
  return fmaxf(__uint_as_float(t[0]), __uint_as_float(t[1]));
}
__device__ __forceinline__ float xrow_sum(float x) {
  auto s = __builtin_amdgcn_permlane16_swap(__float_as_uint(x), __float_as_uint(x), false, false);
  x = __uint_as_float(s[0]) + __uint_as_float(s[1]);
  auto t = __builtin_amdgcn_permlane32_swap(__float_as_uint(x), __float_as_uint(x), false, false);
  return __uint_as_float(t[0]) + __uint_as_float(t[1]);
}
__device__ __forceinline__ u32x4 col_frag(const u32x4* W, int d) {
  u32x4 T;
#pragma unroll
  for (int t = 0; t < 4; ++t) T[t] = __builtin_amdgcn_perm(W[2 * t + 1][d >> 1], W[2 * t][d >> 1], (d & 1) ? 0x07060302u : 0x05040100u);
  return T;
}
#define WAIT_LGKM() asm volatile("s_waitcnt lgkmcnt(0)" ::: "memory")
#define WAIT_VM0() asm volatile("s_waitcnt vmcnt(0)" ::: "memory")
template <int N> __device__ __forceinline__ void waitvm() { asm volatile("s_waitcnt vmcnt(%0)" :: "n"(N)); }
__device__ __forceinline__ unsigned now32() { return (unsigned)__builtin_amdgcn_s_memrealtime(); }
__device__ __forceinline__ unsigned pace_until(unsigned target) {
  unsigned late;
  asm volatile("1:\n\ts_memrealtime vcc\n\ts_waitcnt lgkmcnt(0)\n\ts_sub_u32 %0, vcc_lo, %1\n\ts_cmp_lt_i32 %0, 0\n\ts_cbranch_scc0 2f\n\ts_sleep 1\n\ts_branch 1b\n2:"
               : "=&s"(late) : "s"(target) : "vcc", "scc");
  return late;
}

constexpr unsigned TAU = (unsigned)(1.048576 / RATE * 100 * 256 + 0.5);
constexpr unsigned TAUW = TAU + (unsigned)(TAU * WF / (WL * 16)), TAUP = TAU + TAU * PBS / 48, TAUQ = TAU + TAU * QS / 32;
constexpr int PB0 = 9;
constexpr bool in_win(int n) { return (n >= W1 && n < W1 + WL) || (n >= W2 && n < W2 + WL); }
constexpr unsigned tau_of(int n) { return (n == 1 || n == 2) ? TAUQ : in_win(n) ? TAUW : (n >= PB0 && n < PB0 + 3) ? TAUP : TAU; }
constexpr unsigned start_of(int n) { unsigned t = HEAD * TAU; for (int k = 0; k < n; ++k) t += tau_of(k) * 16; return t / 256; }
constexpr unsigned long long win_mask() { unsigned long long m = 0; for (int n = 0; n < 40; ++n) if (in_win(n)) m |= 1ull << n; return m; }
constexpr unsigned PHMAX = TAU * GG / 256;
static_assert(W1 >= 3 && W1 + WL <= PB0 && W2 >= PB0 + 3, "schedule");

struct Pacer {
  unsigned t0, acc, step, gain, ph, tg, seq;
  int adj, corr, left;
  LAS unsigned* org;
  LAS int* tgt; int prev, wv;
  LAS u32x4* cmd;
  __device__ __forceinline__ unsigned gate() {
    const unsigned t = t0 + ((acc + adj) >> 8);
    const unsigned late = pace_until(t);
    tg = t + late;
    acc += step;
    adj += left > 0 ? corr : 0;
    left -= 1;
    return late;
  }
  __device__ __forceinline__ void gate_first(int n) {
    const bool win = (win_mask() >> n) & 1;
    step = (win ? TAUW : (n == 1 || n == 2) ? TAUQ : (n >= PB0 && n < PB0 + 3) ? TAUP : TAU) * GG;
    gain = (win ? PGW : PG) * (TAU * GG / 256);
    const u32x4 cv = *cmd;
    if (ALIGN) {
      const u32x4 tv = *(LAS u32x4*)(tgt + ((n & 1) ^ 1) * 4);
      adj += (__builtin_amdgcn_readfirstlane((int)(tv[0] + tv[1] + tv[2] + tv[3])) >> 2) - prev;
      prev = adj;
      tgt[(n & 1) * 4 + wv] = adj;
    }
    const unsigned cs = __builtin_amdgcn_readfirstlane(cv[0]), co = __builtin_amdgcn_readfirstlane(cv[1]) + ph, lg = __builtin_amdgcn_readfirstlane(cv[3]) & 7u;
    const int want = (((int)(co - t0) << 8) - adj) >> lg;
    const int hi = TAU * GG * CUP / 256, lo = -(int)(TAU * GG * CDN / 256);
    corr = cs != seq ? (want > hi ? hi : want < lo ? lo : want) : corr;
    left = cs != seq ? 1 << lg : left;
    seq = cs;
    const unsigned o = t0 - ph + (adj >> 8);
    const unsigned late = gate();
    *org = o + (late > 14u ? late - 14u : 0u);
  }
  __device__ __forceinline__ void probe() {
    pace_until(tg + DLY);
    const unsigned ib = __builtin_amdgcn_s_getreg(7 | (31 << 11));
    const unsigned vm = (ib & 15u) | ((ib >> 26) & 0x30u);
    const unsigned e = vm > GG ? vm - GG : 0u;
    adj += (e > 4u ? 4u : e) * gain;
  }
};

constexpr unsigned WS_EPOCH = 0;
constexpr unsigned WS_Q = 4096;
constexpr unsigned WS_PB = WS_Q + 8 * 256 * 128;
constexpr unsigned WS_OUTP = WS_PB + 256 * 2048;
constexpr unsigned WS_SY = WS_OUTP + 256 * 4096;
constexpr unsigned WS_END = WS_SY + 2 * 256 * 128;
#define STAMP(k) do {} while (0)
#define STAMP_DECL do {} while (0)
#define STAMP_FLUSH() do {} while (0)
constexpr int L_QF = 0;
constexpr int L_PB = L_QF + 2048;
constexpr int L_MLB = L_PB + 8192;
constexpr int L_PA = L_MLB + 128;
constexpr int L_MLA = L_PA + 1024;
constexpr int L_GB = L_MLA + 32;
constexpr int L_GML = L_GB + 1024;
constexpr int L_TAG = L_GML + 32;
constexpr int L_SYNC = L_TAG + 16;
constexpr int L_CMD = L_SYNC + 32, L_ORG = L_CMD + 16;
constexpr int L_TGT = L_ORG + 16;
constexpr int L_SC = L_TGT + 32;
constexpr int L_QP = L_SC + 16384;
constexpr int L_USED = L_QP + 16384;
constexpr int L_END = 96 * 1024;
static_assert(L_USED <= L_END, "LDS map");
enum { S_QP_CNT = 0, S_QB_READY = 1, S_QA_READY = 2, S_BP_CNT = 3, S_ML_CNT = 4, S_PB_SEEN = 5 };

__device__ __forceinline__ void lds_wait_ge(LAS unsigned* w, unsigned target) {
#pragma unroll 1
  for (unsigned spins = 0; spins < (1u << 22); ++spins) {
    const unsigned v = __builtin_amdgcn_readfirstlane(__hip_atomic_load(w, __ATOMIC_RELAXED, __HIP_MEMORY_SCOPE_WORKGROUP));
    if (v >= target) break;
    __builtin_amdgcn_s_sleep(1);
  }
  asm volatile("" ::: "memory");
}
__device__ __forceinline__ void lds_signal(LAS unsigned* w, int lane) {
  WAIT_LGKM();
  if (lane == 0) __hip_atomic_fetch_add(w, 1u, __ATOMIC_RELAXED, __HIP_MEMORY_SCOPE_WORKGROUP);
}

constexpr int U = 16;
constexpr unsigned STEP = 65536;
__device__ __forceinline__ float quad_diag(const f32x4 d) { return ((dpp<0x00>(d[0]) + dpp<0x55>(d[1])) + dpp<0xAA>(d[2])) + dpp<0xFF>(d[3]); }
__device__ __forceinline__ unsigned ld_off(const unsigned (&lo)[4], int i) { return lo[i >> 2] + (i & 1) * 4096 + ((i >> 1) & 1) * 32768; }
struct Next { rsrc_t r; unsigned v, so; int n; };
__device__ __forceinline__ void reload(u32x4& R, int i, const Next& nx, const unsigned (&lo)[4], Pacer& pc) {
  if (i == 0) pc.gate_first(nx.n); else if (i % GG == 0) pc.gate();
  R = ld128<AUX_NT>(nx.r, nx.v, nx.so + ld_off(lo, i));
}

__device__ __forceinline__ void k_step(u32x4 (&R)[U], const u32x4 Qf, float& mx, LAS unsigned char* sc, bool writer,
                                       const Next& nx, const unsigned (&lo)[4], Pacer& pc) {
  float s[U];
  f32x4 d[U];
  const f32x4 z = {0, 0, 0, 0};
#pragma unroll
  for (int i = 0; i < U; ++i) {
    d[i] = mfma32(R[i], Qf, z);
    reload(R[i], i, nx, lo, pc);
    if (i > 0) s[i - 1] = quad_diag(d[i - 1]);
    SB();
    if (i % GG == GG - 1 && i < U - 1) { pc.probe(); SB(); }
  }
  s[U - 1] = quad_diag(d[U - 1]);
#pragma unroll
  for (int i = 0; i < U; ++i) mx = fmaxf(mx, s[i]);
  if (writer) {
#pragma unroll
    for (int t = 0; t < 4; ++t) *(LAS f32x4*)(sc + t * 16) = f32x4{s[4 * t], s[4 * t + 1], s[4 * t + 2], s[4 * t + 3]};
  }
  SB();
}
__device__ __forceinline__ void k_step_first(u32x4 (&R)[U], const u32x4 (&X0)[U], const u32x4 (&X1)[U], const u32x4 Qf, float& mx, LAS unsigned char* sc, bool writer,
                                             const Next& nx, const unsigned (&lo)[4], Pacer& pc) {
  const f32x4 z = {0, 0, 0, 0};
#pragma unroll
  for (int t = 0; t < 4; ++t) {
    f32x4 d[4];
#pragma unroll
    for (int u = 0; u < 4; ++u) {
      d[u] = mfma32(R[4 * t + u], Qf, z);
      reload(R[4 * t + u], 4 * t + u, nx, lo, pc);
      SB();
    }
    f32x4 s2, s0, s1;
#pragma unroll
    for (int u = 0; u < 4; ++u) { s2[u] = quad_diag(d[u]); s0[u] = quad_diag(mfma32(X0[4 * t + u], Qf, z)); s1[u] = quad_diag(mfma32(X1[4 * t + u], Qf, z)); }
#pragma unroll
    for (int u = 0; u < 4; ++u) mx = fmaxf(mx, fmaxf(s2[u], fmaxf(s0[u], s1[u])));
    if (writer) { *(LAS f32x4*)(sc + t * 16) = s0; *(LAS f32x4*)(sc + 1024 + t * 16) = s1; *(LAS f32x4*)(sc + 2048 + t * 16) = s2; }
    asm volatile("" : "+v"(mx));
    SB();
  }
}
struct Acc { float l; f32x4 lo, hi; };
__device__ __forceinline__ void p_step(unsigned (&a0)[U], unsigned (&a1)[U], float mc, float& l, LAS unsigned char* sc, unsigned mask0, unsigned mask1) {
  f32x4 s4[4];
#pragma unroll
  for (int t = 0; t < 4; ++t) s4[t] = *(LAS f32x4*)(sc + t * 16);
#pragma unroll
  for (int i = 0; i < U; ++i) {
    const float p = __builtin_amdgcn_exp2f(fmaf(s4[i >> 2][i & 3], CSC, -mc));
    l += p;
    const unsigned t = cvtpk(p, p);
    a0[i] = t & mask0; a1[i] = t & mask1;
  }
#pragma unroll
  for (int i = 0; i < U; ++i) asm volatile("" : "+v"(a0[i]), "+v"(a1[i]));
  asm volatile("" : "+v"(l));
  SB();
}
template <bool RELOAD>
__device__ __forceinline__ void v_step(u32x4 (&R)[U], const unsigned (&a0)[U], const unsigned (&a1)[U], Acc& a,
                                       const Next& nx, const unsigned (&lo)[4], Pacer& pc) {
#pragma unroll
  for (int i = 0; i < U; ++i) {
    a.lo = mfma16(a0[i], a1[i], R[i][0], R[i][1], a.lo);
    a.hi = mfma16(a0[i], a1[i], R[i][2], R[i][3], a.hi);
    if (RELOAD) reload(R[i], i, nx, lo, pc);
    SB();
    if (RELOAD && i % GG == GG - 1 && i < U - 1) { pc.probe(); SB(); }
  }
}
__device__ __forceinline__ void issue_step(u32x4 (&R)[U], const Next& nx, const unsigned (&lo)[4], Pacer& pc) {
#pragma unroll
  for (int i = 0; i < U; ++i) {
    if (i % GG == 0) waitvm<12>();
    reload(R[i], i, nx, lo, pc);
    SB();
    if (i % GG == GG - 1 && i < U - 1) { pc.probe(); SB(); }
  }
}

__device__ __forceinline__ unsigned my_origin(LAS unsigned char* lds) {
  const u32x4 o = *(LAS u32x4*)(lds + L_ORG);
  return o[0] + ((int)(o[1] - o[0]) + (int)(o[2] - o[0]) + (int)(o[3] - o[0])) / 4;
}
__device__ __forceinline__ void agree(LAS unsigned char* lds, unsigned mine, unsigned theirs, bool ok, unsigned seq, unsigned lg, int lane) {
  float d = (ok && lane < 32) ? (float)(int)(theirs - mine) : 0.f;
  d += dpp<0xB1>(d); d += dpp<0x4E>(d); d += dpp<0x141>(d); d += dpp<0x128>(d);
  d = xrow_sum(d);
  const int n = __builtin_popcount((unsigned)__builtin_amdgcn_ballot_w64(ok));
  if (lane == 0) *(LAS u32x4*)(lds + L_CMD) = u32x4{seq, mine + (int)(d * __builtin_amdgcn_rcpf((float)(n > 0 ? n : 1))), 0u, lg};
}
__device__ __forceinline__ void agree_round(LAS unsigned char* lds, rsrc_t rws, unsigned when, int round, unsigned seq, unsigned lg, unsigned tagA, unsigned tagB, int j, int lane) {
  pace_until(when);
  const unsigned mine = my_origin(lds);
  if (lane < 8) st128(u32x4{mine, tagA, mine, tagB}, rws, WS_SY + (round * 256 + j) * 128 + lane * 16);
  pace_until(when + 250);
  const u32x4 f = ld128<AUX_SC1>(rws, (lane & 31) * 128, WS_SY + (round * 256 + (j & ~31)) * 128);
  agree(lds, mine, f[0], f[1] == tagA && f[3] == tagB, seq, lg, lane);
}

template <int WS>
__device__ __forceinline__ void wo_load(u32x4 (&T)[32], const uint16_t* wo, int h, int hw, int g, int c, unsigned th0) {
  constexpr int P = (start_of(WS + WL) - start_of(WS)) / 39;
  const unsigned ts = th0 + start_of(WS) + (h >> 3) * 2 * P + (h & 7) * (P / 8);
  rsrc_t rw = mk(wo + (size_t)h * DH * DM, DH * DM * 2);
  const unsigned vo = (unsigned)(8 * g) * 1024u + hw * 256 + c * 16;
  u32x4 W[32];
#pragma unroll
  for (int i = 0; i < 32; ++i) {
    pace_until(ts + i * P);
    W[i] = ld128<AUX_NT>(rw, vo + (i & 3) * 1024, (32 * (i >> 3) + (i & 4)) * 1024); SB();
  }
  __builtin_amdgcn_s_setprio(0);
#pragma unroll
  for (int s = 0; s < 4; ++s) {
#pragma unroll
    for (int d = 0; d < 8; ++d) T[8 * s + d] = col_frag(W + 8 * s, d);
#pragma unroll
    for (int d = 0; d < 8; ++d) asm volatile("" : "+v"(T[8 * s + d]));
    SB();
  }
  __builtin_amdgcn_s_setprio(3);
}
__device__ __forceinline__ f32x4 merge_weights(const f32x4 m4, const f32x4 l4) {
  const float M = fmaxf(fmaxf(m4[0], m4[1]), fmaxf(m4[2], m4[3]));
  f32x4 w4;
#pragma unroll
  for (int t = 0; t < 4; ++t) w4[t] = __builtin_amdgcn_exp2f(m4[t] - M);
  const float L = (w4[0] * l4[0] + w4[1] * l4[1]) + (w4[2] * l4[2] + w4[3] * l4[3]);
  return w4 * (1.0f / L);
}
__device__ __forceinline__ u32x4 project4(const u32x4 (&A)[4], const u32x4 (&T)[32], const f32x4 w4) {
  f32x4 D[8];
#pragma unroll
  for (int d = 0; d < 8; ++d) D[d] = f32x4{0, 0, 0, 0};
#pragma unroll
  for (int s = 0; s < 4; ++s)
#pragma unroll
    for (int d = 0; d < 8; ++d) D[d] = mfma32(A[s], T[s * 8 + d], D[d]);
  float o[8];
#pragma unroll
  for (int d = 0; d < 8; ++d) o[d] = (D[d][0] * w4[0] + D[d][1] * w4[1]) + (D[d][2] * w4[2] + D[d][3] * w4[3]);
  return u32x4{cvtpk(o[0], o[1]), cvtpk(o[2], o[3]), cvtpk(o[4], o[5]), cvtpk(o[6], o[7])};
}
__device__ __forceinline__ u32x2 tags_of(u32x2 ep) {
  const unsigned long long t = (((unsigned long long)ep[1] << 32) | ep[0]) + 1ull;
  return u32x2{(unsigned)t, (unsigned)(t >> 32) ^ ((unsigned)t * 0x9E3779B1u)};
}

__global__ void __launch_bounds__(512, 2) k_fused(const uint16_t* xA, const uint16_t* KA, const uint16_t* VA, const uint16_t* wqA, const uint16_t* woA,
                                                  const uint16_t* xB, const uint16_t* KB, const uint16_t* VB, const uint16_t* wqB, const uint16_t* woB,
                                                  uint16_t* out, unsigned char* ws) {
  __shared__ __attribute__((aligned(16))) unsigned char smem[L_END];
  LAS unsigned char* lds = (LAS unsigned char*)smem;
  LAS unsigned* sync = (LAS unsigned*)(lds + L_SYNC);
  const int lane = threadIdx.x & 63, wave = __builtin_amdgcn_readfirstlane(threadIdx.x >> 6);
  const int j = blockIdx.x, b = j >> 5, h = j & 31;
  const int g = lane >> 4, c = lane & 15, jj = c & 3;
  const unsigned ph = (j >> 3) * (TAU * GG / 32) / 256;
  STAMP_DECL;
  STAMP(0);
  if (wave < 4) {
    constexpr unsigned AB = 1u << 20, BB = 1u << 18;
    __builtin_amdgcn_s_setprio(2);
    const unsigned kv = ((lane >> 2) & 3) * 256 + (4 * g + (lane & 3)) * 16, vv = lane * 16;
    const rsrc_t rkA = mk((const char*)KA + (size_t)j * AB, AB), rvA = mk((const char*)VA + (size_t)j * AB, AB);
    const rsrc_t rkB = mk((const char*)KB + (size_t)j * BB, BB), rvB = mk((const char*)VB + (size_t)j * BB, BB);
    STAMP(11);
    Pacer pc; pc.t0 = now32() + ph; pc.acc = HEAD * TAU; pc.step = 0; pc.gain = 0; pc.ph = ph; pc.tg = 0; pc.seq = 0;
    pc.adj = 0; pc.corr = 0; pc.left = 0; pc.org = (LAS unsigned*)(lds + L_ORG) + wave; pc.cmd = (LAS u32x4*)(lds + L_CMD);
    {
      const unsigned x = __builtin_amdgcn_raw_buffer_load_b32(wave == 0 ? rkB : wave == 1 ? rvB : wave == 2 ? rkA : rvA, lane == 0 ? 0u : OOB, 0, AUX_NT);
      __builtin_amdgcn_s_barrier();
      asm volatile("" :: "v"(x) : "memory");
      if (ALIGN) pc.t0 = __builtin_amdgcn_readfirstlane(*pc.org) + ph;
      const int over = (int)(now32() - (pc.t0 - ph)) - L0T;
      pc.adj = over > 0 ? over << 8 : 0;
      pc.tgt = (LAS int*)(lds + L_TGT); pc.prev = 0; pc.wv = wave;
      STAMP(13);
    }
    unsigned lo[4];
#pragma unroll
    for (int i = 0; i < 4; ++i) lo[i] = wave * 1024 + ((i ^ ((__builtin_popcount(j & 0x13) & 1) | ((__builtin_popcount(j & 0x2c) & 1) << 1))) << 13);
    u32x4 R[U], X0[U], X1[U];
    issue_step(X0, Next{rkB, kv, 0, 0}, lo, pc);
    issue_step(X1, Next{rkB, kv, STEP, 1}, lo, pc);
    issue_step(R, Next{rkB, kv, 2 * STEP, 2}, lo, pc);
    STAMP(5);
    lds_wait_ge(sync + S_QB_READY, 1);
    u32x4 Qf = *(LAS u32x4*)(lds + L_QF + lane * 16);
    STAMP(1);
    LAS unsigned char* scB = lds + L_SC + wave * 4096 + (g * 4 + (c >> 2)) * 64;
    LAS unsigned char* scA = lds + L_SC + wave * 4096 + g * 64;
    const unsigned mask0 = jj == 0 ? 0x0000ffffu : jj == 1 ? 0xffff0000u : 0u, mask1 = jj == 2 ? 0x0000ffffu : jj == 3 ? 0xffff0000u : 0u;
    float mx = NEG_BIG, mc = 0.f;
    k_step_first(R, X0, X1, Qf, mx, scB, jj == 0, Next{rkB, kv, 3 * STEP, 3}, lo, pc);
    STAMP(16); STAMP(17); STAMP(18);
    Acc a;
    unsigned a0[U], a1[U];
    LAS unsigned char* sc = scB + 3 * 1024;
    int p = 3;
#pragma unroll 1
    for (int br = 0; br < 2; ++br) {
      const int kend = br ? 24 : 4, vend = br ? 39 : 8, sstep = br ? 256 : 1024;
      const bool writer = br ? c == 0 : jj == 0;
      const rsrc_t rv = br ? rvA : rvB;
#pragma unroll 1
      for (; p < kend; ++p) {
        const int n = p + 1;
        const bool last = n == kend;
        k_step(R, Qf, mx, sc, writer, Next{last ? rv : rkA, last ? vv : kv, (unsigned)__builtin_amdgcn_readfirstlane(last ? 0 : (n - 8) * STEP), n}, lo, pc);
        STAMP(16 + p);
        sc += sstep;
      }
      mc = xrow_max(mx) * CSC;
      a.l = 0.f; a.lo = f32x4{0, 0, 0, 0}; a.hi = a.lo;
      sc = br ? scA : scB;
#pragma unroll 1
      for (; p < vend; ++p) {
        const int n = p + 1;
        const bool last = n == 8;
        p_step(a0, a1, mc, a.l, sc, mask0, mask1);
        v_step<true>(R, a0, a1, a, Next{last ? rkA : rv, last ? kv : vv, (unsigned)__builtin_amdgcn_readfirstlane(last ? 0 : (n - kend) * STEP), n}, lo, pc);
        STAMP(16 + p);
        sc += sstep;
      }
      if (br == 0) {
        const float lt = xrow_sum(a.l);
        *(LAS f32x4*)(lds + L_PB + wave * 2048 + lane * 32) = a.lo; *(LAS f32x4*)(lds + L_PB + wave * 2048 + lane * 32 + 16) = a.hi;
        if (g == 0 && jj == 0) { *(LAS float*)(lds + L_MLB + (c >> 2) * 32 + wave * 4) = mc; *(LAS float*)(lds + L_MLB + (c >> 2) * 32 + 16 + wave * 4) = lt; }
        lds_signal(sync + S_BP_CNT, lane);
        STAMP(3);
        lds_wait_ge(sync + S_QA_READY, 1);
        Qf = *(LAS u32x4*)(lds + L_QF + 1024 + lane * 16);
        mx = NEG_BIG; sc = scA;
      }
    }
    p_step(a0, a1, mc, a.l, sc, mask0, mask1);
    {
      const float lt = xrow_sum(a.l);
      if (lane == 0) { *(LAS float*)(lds + L_MLA + wave * 4) = mc; *(LAS float*)(lds + L_MLA + 16 + wave * 4) = lt; }
      lds_signal(sync + S_ML_CNT, lane);
    }
    const rsrc_t rws = mk(ws, WS_END), rout = mk(out, 8 * 1024 * 2);
    unsigned in_off = WS_OUTP + (b * 32 + 8 * g) * 4096 + h * 128 + c * 8;
    unsigned outp = g == 0 ? (b * 1024 + (c >> 3) * 512 + h * 16 + (c & 7) * 2) * 2 : OOB;
    unsigned mytag = *(LAS unsigned*)(lds + L_TAG + (c & 1) * 4);
    u32x2 next_ep = {*(LAS unsigned*)(lds + L_TAG), *(LAS unsigned*)(lds + L_TAG + 8)};
    unsigned ep_off = (h == 0 && lane == 0) ? WS_EPOCH + b * 256 : OOB;
    const unsigned phb = ((j >> 3) | 3) * (TAU * GG / 32) / 256;
    unsigned tp = __builtin_amdgcn_readfirstlane((*pc.cmd)[1]) + (start_of(40) - PHMAX + ENDC) + phb;
    const unsigned tq = pc.tg - ph + phb + ENDO;
    tp = (tp - tq) < 200u ? tp : tq;
    asm volatile("" : "+v"(in_off), "+v"(outp), "+v"(mytag), "+v"(next_ep), "+v"(ep_off));
    SB();
    v_step<false>(R, a0, a1, a, Next{rvA, vv, 0, 0}, lo, pc);
    if (g == 0) *(LAS u32x4*)(lds + L_PA + wave * 256 + c * 16) = u32x4{cvtpk(a.lo[0], a.lo[1]), cvtpk(a.lo[2], a.lo[3]), cvtpk(a.hi[0], a.hi[1]), cvtpk(a.hi[2], a.hi[3])};
    WAIT_LGKM();
    __builtin_amdgcn_s_barrier();
    STAMP(55);
    STAMP(2);
    if (wave == 0) {
      __builtin_amdgcn_s_sleep(TSLEEP);
      pace_until(tp);
      unsigned v[8];
#pragma unroll 1
      for (unsigned spins = 0; spins < (1u << 18); ++spins) {
        u32x2 t[8];
        asm volatile("" : "+v"(in_off) :: "memory");
#pragma unroll
        for (int i = 0; i < 8; ++i) t[i] = __builtin_amdgcn_raw_buffer_load_b64(rws, in_off, i * 4096, AUX_SC1);
        unsigned bad = 0;
#pragma unroll
        for (int i = 0; i < 8; ++i) { v[i] = t[i][0]; bad |= t[i][1] ^ mytag; }
        if (__builtin_amdgcn_ballot_w64(bad != 0) == 0) break;
      }
      float e[8], o[8];
#pragma unroll
      for (int i = 0; i < 8; ++i) { e[i] = __uint_as_float(v[i] << 16); o[i] = __uint_as_float(v[i] & 0xffff0000u); }
      float s0 = ((e[0] + e[1]) + (e[2] + e[3])) + ((e[4] + e[5]) + (e[6] + e[7])), s1 = ((o[0] + o[1]) + (o[2] + o[3])) + ((o[4] + o[5]) + (o[6] + o[7]));
      s0 = xrow_sum(s0); s1 = xrow_sum(s1);
      __builtin_amdgcn_raw_buffer_store_b32(cvtpk(s0, s1), rout, outp, 0, 0);
      __builtin_amdgcn_raw_buffer_store_b64(next_ep, rws, ep_off, 0, AUX_SC1);
      STAMP(12);
    }
    STAMP(9);
  } else {
    const int hw = wave - 4;
    __builtin_amdgcn_s_setprio(3);
    const rsrc_t rws = mk(ws, WS_END);
    asm volatile("" :: "s"(ws));
    SB();
    const unsigned th0 = now32();
    const int pq = (j & 7) * 32 + (j >> 3);
    u32x2 epv;
    {
      const rsrc_t rw = mk(((pq & 128) ? wqB : wqA) + (pq & 126) * 32, (unsigned)(DM * 4096 * 2 - (pq & 126) * 64));
      const rsrc_t rx = mk((pq & 128) ? xB : xA, 8 * DM * 2);
      const unsigned row0 = 128 * hw + 16 * g + 8 * (c >> 3);
      const unsigned vo = row0 * 8192u + (c & 7) * 16;
      const unsigned tw = th0 + ((j >> 4) * QPH) / 16;
      u32x4 W[16];
      pace_until(tw);
#pragma unroll
      for (int k = 0; k < 4; ++k) { const int i = (k >> 1) + (k & 1) * 4; W[i] = ld128<AUX_NT>(rw, vo, i * 8192); SB(); }
      const unsigned xo = (c & 7) * (DM * 2) + row0 * 2;
      const u32x4 A0 = ld128<AUX_NT>(rx, xo, 0), A1 = ld128<AUX_NT>(rx, xo, 128);
      epv = __builtin_amdgcn_raw_buffer_load_b64(rws, (lane >> 3) * 256, WS_EPOCH, AUX_SC1);
      if (hw == 0 && lane < 24) sync[lane] = (lane >> 2) == 3 ? th0 : 0u;
      WAIT_LGKM();
      __builtin_amdgcn_s_barrier();
      SB();
#pragma unroll
      for (int k = 4; k < 16; ++k) {
        if (k % 4 == 0) pace_until(tw + (k / 4) * QPH);
        const int i = (k & 8) + ((k & 7) >> 1) + (k & 1) * 4;
        W[i] = ld128<AUX_NT>(rw, vo, ((i >> 3) * 64 + (i & 7)) * 8192); SB();
      }
      const f32x4 z = {0, 0, 0, 0};
      f32x4 D[8];
#pragma unroll
      for (int d = 0; d < 8; ++d) D[d] = mfma32(A0, col_frag(W, d), z);
#pragma unroll
      for (int d = 0; d < 8; ++d) D[d] = mfma32(A1, col_frag(W + 8, d), D[d]);
      STAMP(25);
      if ((g >> 1) == (c >> 3) && ((c >> 2) & 1) == (pq & 1)) {
#pragma unroll
        for (int t = 0; t < 4; ++t) {
          LAS unsigned char* qp = lds + L_QP + (hw * 2 + (c >> 3)) * 1024 + (4 * (g & 1) + t) * 128 + jj * 32;
          *(LAS f32x4*)qp = f32x4{D[0][t], D[1][t], D[2][t], D[3][t]}; *(LAS f32x4*)(qp + 16) = f32x4{D[4][t], D[5][t], D[6][t], D[7][t]};
        }
      }
    }
    lds_signal(sync + S_QP_CNT, lane);
    const u32x2 tgv = tags_of(epv);
    const unsigned tagA = __builtin_amdgcn_readlane(tgv[0], 8 * b), tagB = __builtin_amdgcn_readlane(tgv[1], 8 * b);
    if (hw == 0) {
      lds_wait_ge(sync + S_QP_CNT, 4);
      f32x4 s[8];
#pragma unroll
      for (int k = 0; k < 8; ++k) s[k] = *(LAS f32x4*)(lds + L_QP + k * 1024 + lane * 16);
      const f32x4 q = ((s[0] + s[1]) + (s[2] + s[3])) + ((s[4] + s[5]) + (s[6] + s[7]));
      st128(u32x4{cvtpk(q[0], q[1]), tgv[0], cvtpk(q[2], q[3]), tgv[1]}, rws, WS_Q + ((lane >> 3) * 256 + pq) * 128 + (lane & 7) * 16);
      if (lane == 8 * b) { *(LAS unsigned*)(lds + L_TAG) = tagA; *(LAS unsigned*)(lds + L_TAG + 4) = tagB; *(LAS unsigned*)(lds + L_TAG + 8) = epv[1] + (tagA == 0u ? 1u : 0u); }
      STAMP(10);
    }
    if (hw == 1 || hw == 2) {
      unsigned qo = WS_Q + (b * 256 + (hw == 1 ? 128 + 4 * ((h & ~3) + (c >> 2)) : 4 * h) + g) * 128 + jj * 32;
      u32x4 f0, f1;
      pace_until(th0 + (hw == 1 ? QPOLL : QPOLL + 200));
#pragma unroll 1
      for (unsigned spins = 0; spins < (1u << 17); ++spins) {
        asm volatile("" : "+v"(qo) :: "memory");
        f0 = ld128<AUX_SC1>(rws, qo, 0); f1 = ld128<AUX_SC1>(rws, qo + 16, 0);
        if (__builtin_amdgcn_ballot_w64(f0[1] != tagA || f0[3] != tagB || f1[1] != tagA || f1[3] != tagB) == 0) break;
        __builtin_amdgcn_s_sleep(16);
      }
      *(LAS u32x4*)(lds + L_QF + (hw - 1) * 1024 + lane * 16) = u32x4{f0[0], f0[2], f1[0], f1[2]};
      lds_signal(sync + (hw == 1 ? S_QB_READY : S_QA_READY), lane);
      STAMP(1);
    }
    if (hw == 3) {
      const char* pt = lane == 0 ? (const char*)out : lane == 1 ? (const char*)woA + (size_t)h * 131072 : (const char*)woB + (size_t)h * 131072;
      const unsigned x = __builtin_nontemporal_load((const __attribute__((address_space(1))) unsigned*)pt);
      asm volatile("" :: "v"(x));
    }
    u32x4 T[32];
    wo_load<W1>(T, woB, h, hw, g, c, th0);
    STAMP(2);
    if (hw == 0) {
      lds_wait_ge(sync + S_BP_CNT, 4);
      STAMP(4);
      const f32x4 m4 = *(LAS f32x4*)(lds + L_MLB + g * 32), l4 = *(LAS f32x4*)(lds + L_MLB + g * 32 + 16);
      const float M = fmaxf(fmaxf(m4[0], m4[1]), fmaxf(m4[2], m4[3]));
      f32x4 a0 = {0, 0, 0, 0}, a1 = a0; float L = 0.f;
#pragma unroll
      for (int w = 0; w < 4; ++w) {
        const float e = __builtin_amdgcn_exp2f(m4[w] - M); L += e * l4[w];
        a0 += e * *(LAS f32x4*)(lds + L_PB + w * 2048 + lane * 32); a1 += e * *(LAS f32x4*)(lds + L_PB + w * 2048 + lane * 32 + 16);
      }
      const u32x4 pk = {cvtpk(a0[0], a0[1]), cvtpk(a0[2], a0[3]), cvtpk(a1[0], a1[1]), cvtpk(a1[2], a1[3])};
      const bool own = g == (h & 3);
      if (!own) st128(pk, rws, WS_PB + j * 2048 + lane * 16);
      const unsigned mine = my_origin(lds);
      WAIT_VM0();
      if (c < 2) st128(c == 0 ? u32x4{__float_as_uint(M), tagA, __float_as_uint(L), tagB} : u32x4{mine, tagA, mine, tagB}, rws, WS_PB + j * 2048 + 1024 + c * 64 + g * 16);
      unsigned mo = (lane & 31) * 2048 + 1024 + ((lane & 32) ? (h & 3) * 16 : 64);
      const bool needed = ((lane ^ j) & 28) == 0;
      u32x4 f;
#pragma unroll 1
      for (unsigned spins = 0; spins < (1u << 17); ++spins) {
        __builtin_amdgcn_s_sleep(64);
        asm volatile("" : "+v"(mo) :: "memory");
        f = ld128<AUX_SC1>(rws, mo, WS_PB + (j & ~31) * 2048);
        if (__builtin_amdgcn_ballot_w64(needed && (f[1] != tagA || f[3] != tagB)) == 0) break;
      }
      asm volatile("" ::: "memory");
      const u32x4 v = ld128<AUX_SC1>(rws, own ? OOB : g * 2048 + (h & 3) * 256 + c * 16, WS_PB + (j & ~3) * 2048);
      agree(lds, mine, f[0], f[1] == tagA && f[3] == tagB, 1u, 5u, lane);
      *(LAS u32x4*)(lds + L_GB + lane * 16) = own ? pk : v;
      if (lane >= 32 && needed) { *(LAS unsigned*)(lds + L_GML + (lane & 3) * 4) = f[0]; *(LAS unsigned*)(lds + L_GML + 16 + (lane & 3) * 4) = f[2]; }
      lds_signal(sync + S_PB_SEEN, lane);
    }
    lds_wait_ge(sync + S_PB_SEEN, 1);
    __builtin_amdgcn_s_setprio(0);
    u32x2 keepB;
    {
      u32x4 A[4];
#pragma unroll
      for (int s = 0; s < 4; ++s) A[s] = *(LAS u32x4*)(lds + L_GB + jj * 256 + g * 16 + s * 64);
      const u32x4 o = project4(A, T, merge_weights(*(LAS f32x4*)(lds + L_GML), *(LAS f32x4*)(lds + L_GML + 16)));
      keepB = (g & 2) ? u32x2{o[2], o[3]} : u32x2{o[0], o[1]};
    }
    asm volatile("" : "+v"(keepB));
    __builtin_amdgcn_s_setprio(3);
    STAMP(5);
    SB();
    wo_load<W2>(T, woA, h, hw, g, c, th0);
    STAMP(6);
    if (hw == 1) {
      if (SY1) agree_round(lds, rws, th0 + start_of(SY1), 0, 2u, 5u, tagA, tagB, j, lane);
      if (SY2) agree_round(lds, rws, th0 + start_of(SY2), 1, 3u, 4u, tagA, tagB, j, lane);
    }
    unsigned op_off = WS_OUTP + j * 4096 + (8 * hw + (c >> 1)) * 128 + (g & 1) * 64 + (c & 1) * 32 + (g >> 1) * 16;
    lds_wait_ge(sync + S_ML_CNT, 4);
    f32x4 w4 = merge_weights(*(LAS f32x4*)(lds + L_MLA), *(LAS f32x4*)(lds + L_MLA + 16));
    asm volatile("" : "+v"(w4), "+v"(op_off));
    SB();
    __builtin_amdgcn_s_barrier();
    asm volatile("" ::: "memory");
    STAMP(7);
    {
      u32x4 A[4];
#pragma unroll
      for (int s = 0; s < 4; ++s) A[s] = *(LAS u32x4*)(lds + L_PA + jj * 256 + g * 16 + s * 64);
      SB();
      const u32x4 o = project4(A, T, w4);
      const u32x2 keepA = (g & 2) ? u32x2{o[2], o[3]} : u32x2{o[0], o[1]};
      const u32x2 k = (g & 1) ? keepB : keepA;
      st128(u32x4{k[0], tagA, k[1], tagB}, rws, op_off);
    }
    STAMP(8);
  }
  STAMP_FLUSH();
}

extern "C" void kernel_launch(void* const* d_in, const int* in_sizes, int n_in, void* d_out, int out_size, void* d_ws, size_t ws_size, hipStream_t stream) {
  k_fused<<<256, 512, 0, stream>>>((const uint16_t*)d_in[0], (const uint16_t*)d_in[1], (const uint16_t*)d_in[2], (const uint16_t*)d_in[3], (const uint16_t*)d_in[4],
                                   (const uint16_t*)d_in[5], (const uint16_t*)d_in[6], (const uint16_t*)d_in[7], (const uint16_t*)d_in[8], (const uint16_t*)d_in[9],
                                   (uint16_t*)d_out, (unsigned char*)d_ws);
}
```
